```python
import torch

def _softmax(x, axis=-1):
    return torch.softmax(x, dim=axis)
def _gelu(x):
    return torch.nn.functional.gelu(x, approximate="tanh")
def _silu(x):
    return torch.nn.functional.silu(x)
def _sigmoid(x):
    return torch.sigmoid(x)
def _layernorm(x, axis=-1, eps=1e-5):
    m = x.mean(dim=axis, keepdim=True)
    v = x.var(dim=axis, keepdim=True, unbiased=False)
    return (x - m) / torch.sqrt(v + eps)
def _scatter_add(tgt, idx, src, axis=0):
    return tgt.clone().index_add_(axis, idx, src)
def _segment_sum(src, seg, n):
    out = torch.zeros((n,) + src.shape[1:], dtype=src.dtype)
    return out.index_add_(0, seg, src)
def _pad(x, pw):
    flat = tuple(v for pair in reversed(pw) for v in pair)
    return torch.nn.functional.pad(x, flat)

kernel_name = 'gated_mix__compressed_kv_cla__sliding_block_cached__decode'

def setup_inputs():

    _w = (lambda a: (lambda t: t.to(torch.bfloat16) if t.is_floating_point() else t)(torch.as_tensor(a)))
    def _A_setup():
        B, H, Sq, Sk, D, M = 8, 32, 1, 4096, 128, 512
        Dc = 4 * D
        return {
            "x": _w((lambda *s: torch.randn(*s, dtype=torch.bfloat16) * 0.05)(B, Sq, M)),
            "c_kv": _w((lambda *s: torch.randn(*s, dtype=torch.bfloat16) * 0.4)(B, Sk, Dc)),
            "w_q": _w((lambda *s: torch.randn(*s, dtype=torch.bfloat16) * 6.4)(M, H, D) * (M ** -0.5)),
            "w_uk": _w((lambda *s: torch.randn(*s, dtype=torch.bfloat16) * 6.4)(Dc, H, D) * (Dc ** -0.5)),
            "w_uv": _w((lambda *s: torch.randn(*s, dtype=torch.bfloat16) * 0.4)(Dc, H, D) * (Dc ** -0.5)),
            "w_o": _w((lambda *s: torch.randn(*s, dtype=torch.bfloat16) * 0.4)(H, D, M) * ((H * D) ** -0.5)),
        }
    _A_in = _A_setup()

    def _B_setup():
        B, Sq, D = 8, 1, 512
        Hq, Hkv, Sk, Dh = 32, 8, 4096, 128
        assert Hq % Hkv == 0, (Hq, Hkv)
        return {
            "x": _w((lambda *s: torch.randn(*s, dtype=torch.bfloat16) * 0.05)(B, Sq, D)),
            "k_cache": _w((lambda *s: torch.randn(*s, dtype=torch.bfloat16) * 6.4)(B, Hkv, Sk, Dh)),
            "v_cache": _w((lambda *s: torch.randn(*s, dtype=torch.bfloat16) * 0.05)(B, Hkv, Sk, Dh)),
            "wq": _w((lambda *s: torch.randn(*s, dtype=torch.bfloat16) * 6.4)(D, Hq * Dh) * (D ** -0.5)),
            "wo": _w((lambda *s: torch.randn(*s, dtype=torch.bfloat16) * 0.05)(Hq * Dh, D) * ((Hq * Dh) ** -0.5)),
        }
    _B_in = _B_setup()

    return {
        "A_x": _A_in["x"],
        "A_c_kv": _A_in["c_kv"],
        "A_w_q": _A_in["w_q"],
        "A_w_uk": _A_in["w_uk"],
        "A_w_uv": _A_in["w_uv"],
        "A_w_o": _A_in["w_o"],
        "B_x": _B_in["x"],
        "B_k_cache": _B_in["k_cache"],
        "B_v_cache": _B_in["v_cache"],
        "B_wq": _B_in["wq"],
        "B_wo": _B_in["wo"],
        "gate_w": _w((lambda *s: torch.randn(*s, dtype=torch.bfloat16) * 0.05)(512) * 16.0),
    }

def reference(A_x, A_c_kv, A_w_q, A_w_uk, A_w_uv, A_w_o, B_x, B_k_cache, B_v_cache, B_wq, B_wo, gate_w):
    def _A_ref(x, c_kv, w_q, w_uk, w_uv, w_o):
        B, Sq, M = x.shape[0], x.shape[1], x.shape[2]
        Sk = c_kv.shape[1]
        H, D = w_q.shape[1], w_q.shape[2]
        q = torch.einsum("bqm,mhd->bhqd", x, w_q)
        k = torch.einsum("bkc,chd->bhkd", c_kv, w_uk)
        v = torch.einsum("bkc,chd->bhkd", c_kv, w_uv)
        scores = torch.einsum("bhqd,bhkd->bhqk", q, k) * (D ** -0.5)
        off = Sk - Sq
        qi = torch.arange(Sq)[:, None]
        kj = torch.arange(Sk)[None, :]
        cmask = kj <= (qi + off)
        scores = torch.where(cmask[None, None], scores, -1e9)
        p = _softmax(scores, axis=-1)
        ctx = torch.einsum("bhqk,bhkd->bhqd", p, v)
        return torch.einsum("bhqd,hdm->bqm", ctx, w_o)

    def _B_ref(x, k_cache, v_cache, wq, wo):
        B, Sq, D = x.shape
        Hq, Dh = 32, 128
        Hkv, Sk = k_cache.shape[1], k_cache.shape[2]
        W = min(4096, max(256, Sk // 4))
        rep = Hq // Hkv
        q = (lambda x, *d: x.permute(*d))((x @ wq).reshape(B, Sq, Hq, Dh), 0, 2, 1, 3)
        k = (lambda x, n, axis: torch.repeat_interleave(x, n, dim=axis))(k_cache[:, :, Sk - W:, :], rep, axis=1)
        v = (lambda x, n, axis: torch.repeat_interleave(x, n, dim=axis))(v_cache[:, :, Sk - W:, :], rep, axis=1)
        scores = torch.einsum("bhqd,bhkd->bhqk", q, k) * (Dh ** -0.5)
        p = _softmax(scores, axis=-1)
        o = torch.einsum("bhqk,bhkd->bhqd", p, v)
        return (lambda x, *d: x.permute(*d))(o, 0, 2, 1, 3).reshape(B, Sq, Hq * Dh) @ wo

    x = A_x
    g = _sigmoid((x * gate_w).sum(-1, keepdims=True))
    a = _A_ref(x, A_c_kv, A_w_q, A_w_uk, A_w_uv, A_w_o)
    b = _B_ref(x, B_k_cache, B_v_cache, B_wq, B_wo)
    return g * a + (1 - g) * b
```

```cpp
#include <hip/hip_runtime.h>
#include <hip/hip_bf16.h>
#include <stdint.h>
typedef unsigned short u16;
__device__ __forceinline__ float bf2f(u16 v){ return __uint_as_float(((unsigned)v)<<16); }
__device__ __forceinline__ u16 f2bf(float f){ unsigned u=__float_as_uint(f); u += 0x7fffu + ((u>>16)&1u); return (u16)(u>>16); }
__device__ __forceinline__ float rbf(float f){ return bf2f(f2bf(f)); }

__global__ void k_q(const u16* x, const u16* w, float* q){
  int n = blockIdx.x*blockDim.x+threadIdx.x; int b = blockIdx.y;
  float s=0; for(int m=0;m<512;m++) s += bf2f(x[b*512+m])*bf2f(w[(size_t)m*4096+n]);
  q[b*4096+n]=rbf(s);
}
__global__ void k_qabs(const float* q, const u16* wuk, float* qa){
  int c = threadIdx.x; int h = blockIdx.x, b = blockIdx.y;
  float s=0; for(int d=0;d<128;d++) s += q[b*4096+h*128+d]*bf2f(wuk[((size_t)c*32+h)*128+d]);
  qa[(b*32+h)*512+c]=s*0.08838834764831845f;
}
__global__ void k_attnA(const float* qa, const u16* ckv, float* ctxc){
  __shared__ float sc[4096]; __shared__ float red[512]; __shared__ float qs[512];
  int h=blockIdx.x,b=blockIdx.y,t=threadIdx.x;
  qs[t]=qa[(b*32+h)*512+t]; __syncthreads();
  float mx=-1e30f;
  for(int k=t;k<4096;k+=512){ float s=0; const u16* r=ckv+((size_t)b*4096+k)*512; for(int c=0;c<512;c++) s+=qs[c]*bf2f(r[c]); sc[k]=s; mx=fmaxf(mx,s);}
  red[t]=mx; __syncthreads(); for(int o=256;o>0;o>>=1){ if(t<o) red[t]=fmaxf(red[t],red[t+o]); __syncthreads(); }
  mx=red[0]; __syncthreads(); float l=0; for(int k=t;k<4096;k+=512){ float p=__expf(sc[k]-mx); sc[k]=p; l+=p; }
  red[t]=l; __syncthreads(); for(int o=256;o>0;o>>=1){ if(t<o) red[t]+=red[t+o]; __syncthreads(); }
  l=red[0]; float a=0; for(int k=0;k<4096;k++) a+=sc[k]*bf2f(ckv[((size_t)b*4096+k)*512+t]);
  ctxc[(b*32+h)*512+t]=a/l;
}
__global__ void k_ctx(const float* ctxc, const u16* wuv, float* ctx){ int d=threadIdx.x,h=blockIdx.x,b=blockIdx.y; float s=0; for(int c=0;c<512;c++) s+=ctxc[(b*32+h)*512+c]*bf2f(wuv[((size_t)c*32+h)*128+d]); ctx[b*4096+h*128+d]=s; }
__global__ void k_attnB(const float* q, const u16* K, const u16* V, float* o){
  __shared__ float sc[1024]; __shared__ float red[128]; __shared__ float qs[128];
  int h=blockIdx.x,b=blockIdx.y,t=threadIdx.x; int kv=h/4;
  qs[t]=q[b*4096+h*128+t]; __syncthreads();
  const u16* Kb=K+(((size_t)b*8+kv)*4096+3072)*128; const u16* Vb=V+(((size_t)b*8+kv)*4096+3072)*128;
  float mx=-1e30f; for(int k=t;k<1024;k+=128){ float s=0; for(int d=0;d<128;d++) s+=qs[d]*bf2f(Kb[k*128+d]); s*=0.08838834764831845f; sc[k]=s; mx=fmaxf(mx,s);}
  red[t]=mx; __syncthreads(); for(int o2=64;o2>0;o2>>=1){ if(t<o2) red[t]=fmaxf(red[t],red[t+o2]); __syncthreads(); }
  mx=red[0]; __syncthreads(); float l=0; for(int k=t;k<1024;k+=128){ float p=__expf(sc[k]-mx); sc[k]=p; l+=p; }
  red[t]=l; __syncthreads(); for(int o2=64;o2>0;o2>>=1){ if(t<o2) red[t]+=red[t+o2]; __syncthreads(); }
  l=red[0]; float a=0; for(int k=0;k<1024;k++) a+=sc[k]*bf2f(Vb[k*128+t]);
  o[b*4096+h*128+t]=a/l;
}
__global__ void k_out(const u16* x, const u16* gw, const float* ctx, const float* o, const u16* wo_a, const u16* wo_b, u16* out){
  int m=threadIdx.x+blockIdx.x*64, b=blockIdx.y;
  float gs=0; for(int i=0;i<512;i++) gs+=bf2f(x[b*512+i])*bf2f(gw[i]);
  float g=1.f/(1.f+__expf(-gs));
  float a=0,bb=0; for(int k=0;k<4096;k++){ a+=ctx[b*4096+k]*bf2f(wo_a[(size_t)k*512+m]); bb+=o[b*4096+k]*bf2f(wo_b[(size_t)k*512+m]); }
  out[b*512+m]=f2bf(g*a+(1.f-g)*bb);
}
#define NAIVE_NAME kernel_launch
extern "C" void NAIVE_NAME(void* const* d_in, const int* in_sizes, int n_in, void* d_out, int out_size, void* d_ws, size_t ws_size, hipStream_t stream){
  const u16* x=(const u16*)d_in[0]; const u16* ckv=(const u16*)d_in[1]; const u16* wq=(const u16*)d_in[2]; const u16* wuk=(const u16*)d_in[3];
  const u16* wuv=(const u16*)d_in[4]; const u16* wo=(const u16*)d_in[5]; const u16* K=(const u16*)d_in[7]; const u16* V=(const u16*)d_in[8];
  const u16* wqb=(const u16*)d_in[9]; const u16* wob=(const u16*)d_in[10]; const u16* gw=(const u16*)d_in[11];
  float* ws=(float*)d_ws; float* qA=ws; float* qB=ws+32768; float* qa=ws+65536; float* ctxc=qa+131072; float* ctx=ctxc+131072; float* o=ctx+32768;
  k_q<<<dim3(16,8),256,0,stream>>>(x,wq,qA);
  k_q<<<dim3(16,8),256,0,stream>>>(x,wqb,qB);
  k_qabs<<<dim3(32,8),512,0,stream>>>(qA,wuk,qa);
  k_attnA<<<dim3(32,8),512,0,stream>>>(qa,ckv,ctxc);
  k_ctx<<<dim3(32,8),128,0,stream>>>(ctxc,wuv,ctx);
  k_attnB<<<dim3(32,8),128,0,stream>>>(qB,K,V,o);
  k_out<<<dim3(8,8),64,0,stream>>>(x,gw,ctx,o,wo,wob,(u16*)d_out);
}
```

```cpp
#include <hip/hip_runtime.h>
#include <stdint.h>

typedef unsigned u32;
typedef unsigned u32x2 __attribute__((ext_vector_type(2)));
typedef unsigned u32x4 __attribute__((ext_vector_type(4)));
typedef float f32x2 __attribute__((ext_vector_type(2)));
typedef float f32x4 __attribute__((ext_vector_type(4)));
typedef float f32x16 __attribute__((ext_vector_type(16)));
typedef __bf16 bf16x2 __attribute__((ext_vector_type(2)));
typedef __bf16 bf16x8 __attribute__((ext_vector_type(8)));
typedef short s16x4 __attribute__((ext_vector_type(4)));
#define LAS __attribute__((address_space(3)))
#define DI __device__ __forceinline__
#define ROLE_OF(b) (((b) >> 3) & 1)

#define DA 4
#define QI 12
#define DB 5
#define QBI 12

constexpr u32 WS_EPOCH = 0;
constexpr u32 WS_QABS = 4096;
constexpr u32 WS_QB = WS_QABS + 524288;
constexpr u32 WS_STATS = WS_QB + 1048576;
constexpr u32 WS_PART = WS_STATS + 65536;
constexpr u32 WS_CTXP = WS_PART + 4194304;
constexpr u32 WS_OB = WS_CTXP + 1048576;
constexpr u32 WS_OBS = WS_OB + 524288;
constexpr u32 WS_OUTP = WS_OBS + 8192;
constexpr u32 WS_END = WS_OUTP + 1048576;
constexpr int LDS_BYTES = 163840;
constexpr u32 SCR = 131072;
constexpr int SPIN_CAP = 1 << 15;
constexpr float C2 = 0.08838834764831845f * 1.4426950408889634f;

struct Params {
  const unsigned char *x, *ckv, *wq_a, *wuk, *wuv, *wo_a, *kc, *vc, *wq_b, *wo_b, *gw;
  unsigned char* ws;
  unsigned short* out;
};

#define WAITV(n) asm volatile("s_waitcnt vmcnt(%0)" ::"i"(n) : "memory")
#define BAR() asm volatile("s_waitcnt lgkmcnt(0)\n\ts_barrier" ::: "memory")
#define SB() __builtin_amdgcn_sched_barrier(0)

DI void dma_nt(const unsigned char* sbase, u32 voff, u32 lds) {
  u32 keep;
  asm volatile("s_mov_b32 %0, m0\n\ts_mov_b32 m0, %3\n\ts_nop 0\n\tglobal_load_lds_dwordx4 %1, %2 nt\n\ts_mov_b32 m0, %0"
               : "=&s"(keep) : "v"(voff), "s"(sbase), "s"(lds) : "memory");
}
DI void dma_df(const unsigned char* sbase, u32 voff, u32 lds) {
  u32 keep;
  asm volatile("s_mov_b32 %0, m0\n\ts_mov_b32 m0, %3\n\ts_nop 0\n\tglobal_load_lds_dwordx4 %1, %2\n\ts_mov_b32 m0, %0"
               : "=&s"(keep) : "v"(voff), "s"(sbase), "s"(lds) : "memory");
}
#define LD_NT(dst, voff, sbase, imm) asm volatile("global_load_dwordx4 %0, %1, %2 offset:%3 nt" : "=v"(dst) : "v"(voff), "s"(sbase), "i"(imm) : "memory")
#define LD_SC1(dst, voff, sbase, imm) asm volatile("global_load_dwordx4 %0, %1, %2 offset:%3 sc1" : "=v"(dst) : "v"(voff), "s"(sbase), "i"(imm) : "memory")
#define LD_DF(dst, voff, sbase, imm) asm volatile("global_load_dwordx4 %0, %1, %2 offset:%3" : "=v"(dst) : "v"(voff), "s"(sbase), "i"(imm) : "memory")
DI void st_sc1(unsigned char* sbase, u32 voff, u32x4 v) {
  asm volatile("global_store_dwordx4 %0, %1, %2 sc1\n\ts_nop 1" ::"v"(voff), "v"(v), "s"(sbase) : "memory");
}
#define PIN4(n, a, b, c, d) asm volatile("s_waitcnt vmcnt(%4) ; PIN %0 %1 %2 %3" : "+v"(a), "+v"(b), "+v"(c), "+v"(d) : "i"(n) : "memory")
#define PIN2(n, a, b) asm volatile("s_waitcnt vmcnt(%2) ; PIN %0 %1" : "+v"(a), "+v"(b) : "i"(n) : "memory")

DI u32x4 lds_r128(u32 a) { return *(LAS u32x4*)a; }
DI u32x2 lds_r64(u32 a) { return *(LAS u32x2*)a; }
DI void lds_w128(u32 a, u32x4 v) { *(LAS u32x4*)a = v; }
DI void lds_w64(u32 a, u32x2 v) { *(LAS u32x2*)a = v; }
DI void lds_w32(u32 a, u32 v) { *(LAS u32*)a = v; }
DI u32x2 lds_tr(u32 a) { return __builtin_bit_cast(u32x2, __builtin_amdgcn_ds_read_tr16_b64_v4i16((LAS s16x4*)a)); }
DI u32x4 cat(u32x2 a, u32x2 b) { return u32x4{a[0], a[1], b[0], b[1]}; }
DI f32x16 mfma32(u32x4 a, u32x4 b, f32x16 c) { return __builtin_amdgcn_mfma_f32_32x32x16_bf16(__builtin_bit_cast(bf16x8, a), __builtin_bit_cast(bf16x8, b), c, 0, 0, 0); }
DI f32x4 mfma16(u32x4 a, u32x4 b, f32x4 c) { return __builtin_amdgcn_mfma_f32_16x16x32_bf16(__builtin_bit_cast(bf16x8, a), __builtin_bit_cast(bf16x8, b), c, 0, 0, 0); }
DI u32 pk(float lo, float hi) { return __builtin_bit_cast(u32, __builtin_convertvector(f32x2{lo, hi}, bf16x2)); }
DI float blo(u32 v) { return __uint_as_float(v << 16); }
DI float bhi(u32 v) { return __uint_as_float(v & 0xffff0000u); }
DI float asf(u32 v) { return __uint_as_float(v); }
DI u32 asu(float v) { return __float_as_uint(v); }
DI float ex2(float v) { return __builtin_amdgcn_exp2f(v); }
DI float x32max(float v) { auto t = __builtin_amdgcn_permlane32_swap(asu(v), asu(v), false, false); return fmaxf(asf(t[0]), asf(t[1])); }
DI float x32sum(float v) { auto t = __builtin_amdgcn_permlane32_swap(asu(v), asu(v), false, false); return asf(t[0]) + asf(t[1]); }
DI float x16max(float v) { auto t = __builtin_amdgcn_permlane16_swap(asu(v), asu(v), false, false); return fmaxf(asf(t[0]), asf(t[1])); }
DI float x16sum(float v) { auto t = __builtin_amdgcn_permlane16_swap(asu(v), asu(v), false, false); return asf(t[0]) + asf(t[1]); }
template <int CTRL> DI float dppf(float v) { return __builtin_bit_cast(float, __builtin_amdgcn_mov_dpp(__builtin_bit_cast(int, v), CTRL, 0xf, 0xf, true)); }
DI bool tag_ok(u32x4 v, u32 tag) { return v[1] == tag && v[3] == tag; }
DI bool all_lanes(bool ok) { return __builtin_amdgcn_ballot_w64(!ok) == 0ull; }
DI u32 fsw(u32 row) { return ((row & 3) << 2) | ((row >> 2) & 3); }

#define STAMP(i)
#define STAMP_FLUSH()

DI f32x4 gemv128(u32 wimg, u32 act, int w, int lane, f32x4 acc) {
  const u32 g = lane >> 4, q = (lane & 15) >> 2, pp = lane & 3, b = lane & 7;
#pragma unroll
  for (int s = 0; s < 4; ++s) {
    u32x2 a[2];
#pragma unroll
    for (int t = 0; t < 2; ++t) {
      const u32 row = 8 * g + 4 * t + q;
      a[t] = lds_tr(wimg + s * 8192 + row * 256 + (((2 * w + (pp >> 1)) ^ fsw(row)) << 4) + 8 * (pp & 1));
    }
    const u32x4 bf = lds_r128(act + b * 256 + (((4 * s + g) ^ (b << 1)) << 4));
    acc = mfma16(cat(a[0], a[1]), bf, acc);
  }
  return acc;
}

__global__ __launch_bounds__(512, 2) void fused_decode(Params p) {
  extern __shared__ __attribute__((aligned(16))) unsigned char smem[];
  const int blk = blockIdx.x, tid = threadIdx.x, lane = tid & 63;
  const int w = __builtin_amdgcn_readfirstlane(tid >> 6);
  const int xcd = blk & 7, i16 = blk >> 4;
  const int h = xcd * 4 + (i16 >> 2), j = i16 & 3;
  const u32 L0 = (u32)(size_t)(LAS unsigned char*)smem;
  const u32 g = lane >> 4;
  STAMP(0);
  const u32 tag = *(const u32*)(p.ws + WS_EPOCH) * 1664525u + 1013904223u;
  unsigned char* const ws = p.ws;
  const u32 pc_chunk = ((lane & 15) ^ ((g << 2) | (w & 3))) << 4;

  if (ROLE_OF(blk) == 0) {
    const int b = xcd, s = i16;
    const unsigned char* cb = p.ckv + ((size_t)(b * 4096 + s * 256 + w)) * 1024;
    u32 vo[2];
    vo[0] = (lane ^ (((w & 3) << 2) | ((w >> 2) & 3))) << 4;
    vo[1] = (lane ^ (((w & 3) << 2) | ((2 + (w >> 2)) & 3))) << 4;
    const unsigned char* uvb = p.wuv + (size_t)(128 * j + 4 * w) * 8192 + h * 256;
    const u32 uv_vo = g * 8192 + pc_chunk;
    const u32 wo_vo = g * 1024 + pc_chunk;
    const size_t wo_off = (size_t)(h * 128 + 4 * w) * 1024 + j * 256;
#define A_ROW(i) do { if ((i) < 32) dma_nt(cb + (size_t)(i) * 8192, vo[(i) & 1], L0 + ((((i) & 15) * 8 + w) << 10)); \
                      else if ((i) < 36) dma_nt(uvb + (size_t)((i) - 32) * 32 * 8192, uv_vo, L0 + ((((i) - 32) * 8 + w) << 10)); } while (0)
    u32x4 qv[8];
    const u32 q_vo = WS_QABS + b * 65536 + w * 8192 + lane * 16;
#define A_QLOAD() do { LD_SC1(qv[0], q_vo, ws, 0); LD_SC1(qv[1], q_vo, ws, 1024); LD_SC1(qv[2], q_vo, ws, 2048); LD_SC1(qv[3], q_vo, ws, 3072); \
                       LD_SC1(qv[4], q_vo2, ws, 0); LD_SC1(qv[5], q_vo2, ws, 1024); LD_SC1(qv[6], q_vo2, ws, 2048); LD_SC1(qv[7], q_vo2, ws, 3072); } while (0)
    const u32 q_vo2 = q_vo + 4096;
#pragma unroll
    for (int i = 0; i < 16; ++i) {
      if (i == QI) A_QLOAD();
      if (i >= DA) { if (i >= QI && i - DA < QI) WAITV(DA - 1 + 8); else WAITV(DA - 1); }
      A_ROW(i);
    }
    if (QI >= 16) A_QLOAD();
    STAMP(1);
    PIN4(QI >= 16 ? 0 : 16 - QI, qv[0], qv[1], qv[2], qv[3]);
    PIN4(QI >= 16 ? 0 : 16 - QI, qv[4], qv[5], qv[6], qv[7]);
    STAMP(2);
    for (int spin = 0; spin < SPIN_CAP; ++spin) {
      bool ok = true;
#pragma unroll
      for (int i = 0; i < 8; ++i) ok = ok && tag_ok(qv[i], tag);
      if (all_lanes(ok)) break;
      __builtin_amdgcn_s_sleep(2);
      A_QLOAD();
      PIN4(0, qv[0], qv[1], qv[2], qv[3]);
      PIN4(0, qv[4], qv[5], qv[6], qv[7]);
    }
    STAMP(3);
#pragma unroll
    for (int i = 0; i < 8; ++i) {
      const u32 hd = 4 * w + (i >> 1), ch = 32 * (i & 1) + (lane >> 1);
      lds_w64(L0 + SCR + hd * 1024 + ((ch ^ fsw(hd)) << 4) + 8 * (lane & 1), u32x2{qv[i][0], qv[i][2]});
    }
    WAITV(8);
    BAR();
    const int kq = w & 3, tl = w >> 2;
    const u32 hh = lane >> 5, r31 = lane & 31;
    u32x4 qf[8];
#pragma unroll
    for (int k = 0; k < 8; ++k) qf[k] = lds_r128(L0 + SCR + r31 * 1024 + (((16 * kq + 2 * k + hh) ^ fsw(r31)) << 4));
    const u32 la = ((r31 >> 3) + kq) & 3;
    const u32 krow = 16 * (la >> 1) + 8 * ((r31 >> 2) & 1) + 4 * (la & 1) + (r31 & 3);
    const u32 qk_base = L0 + (tl * 32 + krow) * 1024 + kq * 256;
    const u32 qk_f = fsw(krow);
    const u32 tq = (lane & 15) >> 2, tp = lane & 3, tb = (lane >> 4) & 1;
    f32x16 o0, o1;
#pragma unroll
    for (int i = 0; i < 16; ++i) { o0[i] = 0.f; o1[i] = 0.f; }
    float m_run = -INFINITY, l_run = 0.f;
    constexpr u32 XB = SCR, PB = SCR + 24576, MXB = SCR + 28672, LB = SCR + 29696;
#pragma unroll
    for (int k = 0; k < 4; ++k) {
      const u32 half = (k & 1) * 65536;
      if (k > 0) {
        const int lo = (k == 1) ? 16 : 8 * k + DA, hi = (k == 3) ? 36 : 8 * (k + 1) + DA;
#pragma unroll
        for (int i = lo; i < hi; ++i) { WAITV(DA - 1); A_ROW(i); }
        WAITV(hi - 8 * (k + 1));
        if (k > 1) BAR();
      }
      STAMP(4 + k);
      f32x16 sa;
#pragma unroll
      for (int i = 0; i < 16; ++i) sa[i] = 0.f;
      if (k == 1) BAR();
#pragma unroll
      for (int kk = 0; kk < 8; ++kk) {
        const u32x4 a = lds_r128(qk_base + half + (((2 * kk + hh) ^ qk_f) << 4));
        sa = mfma32(a, qf[kk], sa);
      }
      if (k == 0) BAR();
#pragma unroll
      for (int a = 1; a < 4; ++a)
        lds_w128(L0 + XB + ((tl * 4 + ((kq + a) & 3)) * 3 + (a - 1)) * 1024 + lane * 16,
                 u32x4{asu(sa[4 * a]), asu(sa[4 * a + 1]), asu(sa[4 * a + 2]), asu(sa[4 * a + 3])});
      BAR();
      float sv[4] = {sa[0], sa[1], sa[2], sa[3]};
#pragma unroll
      for (int a = 0; a < 3; ++a) {
        const u32x4 t = lds_r128(L0 + XB + (w * 3 + a) * 1024 + lane * 16);
#pragma unroll
        for (int e = 0; e < 4; ++e) sv[e] += asf(t[e]);
      }
      float mx = x32max(fmaxf(fmaxf(sv[0], sv[1]), fmaxf(sv[2], sv[3])));
      if (lane < 32) lds_w32(L0 + MXB + r31 * 32 + w * 4, asu(mx));
      BAR();
      {
        const u32x4 t0 = lds_r128(L0 + MXB + r31 * 32), t1 = lds_r128(L0 + MXB + r31 * 32 + 16);
        mx = fmaxf(fmaxf(fmaxf(asf(t0[0]), asf(t0[1])), fmaxf(asf(t0[2]), asf(t0[3]))), fmaxf(fmaxf(asf(t1[0]), asf(t1[1])), fmaxf(asf(t1[2]), asf(t1[3]))));
      }
      const float m_new = fmaxf(m_run, mx);
      const float alpha = ex2(m_run - m_new);
      m_run = m_new;
      const float p0 = ex2(sv[0] - m_new), p1 = ex2(sv[1] - m_new), p2 = ex2(sv[2] - m_new), p3 = ex2(sv[3] - m_new);
      l_run = l_run * alpha + ((p0 + p1) + (p2 + p3));
      lds_w64(L0 + PB + (tl * 2 + (kq >> 1)) * 1024 + lane * 16 + (kq & 1) * 8, u32x2{pk(p0, p1), pk(p2, p3)});
      if (k == 3) { const float lt = x32sum(l_run); if (lane < 32) lds_w32(L0 + LB + r31 * 32 + w * 4, asu(lt)); }
      if (k > 0) {
#pragma unroll
        for (int i = 0; i < 16; ++i) { o0[i] *= alpha; o1[i] *= alpha; }
      }
      BAR();
#pragma unroll
      for (int ks = 0; ks < 4; ++ks) {
        const u32x4 pf = lds_r128(L0 + PB + ks * 1024 + lane * 16);
        u32x2 v[2][2];
#pragma unroll
        for (int ci = 0; ci < 2; ++ci)
#pragma unroll
          for (int t = 0; t < 2; ++t) {
            const u32 row = 8 * hh + 4 * t + tq;
            v[ci][t] = lds_tr(L0 + half + ks * 16384 + row * 1024 + (((8 * w + 4 * ci + 2 * tb + (tp >> 1)) ^ fsw(row)) << 4) + 8 * (tp & 1));
          }
        o0 = mfma32(cat(v[0][0], v[0][1]), pf, o0);
        o1 = mfma32(cat(v[1][0], v[1][1]), pf, o1);
      }
      BAR();
    }
    STAMP(8);
    float l_tot;
    {
      const u32x4 t0 = lds_r128(L0 + LB + r31 * 32), t1 = lds_r128(L0 + LB + r31 * 32 + 16);
      l_tot = ((asf(t0[0]) + asf(t0[1])) + (asf(t0[2]) + asf(t0[3]))) + ((asf(t1[0]) + asf(t1[1])) + (asf(t1[2]) + asf(t1[3])));
    }
    const float inv_l = 1.f / l_tot;
    constexpr u32 TB = 65536;
#pragma unroll
    for (int a = 0; a < 4; ++a) {
      lds_w64(L0 + TB + r31 * 1024 + (((8 * w + a) ^ (r31 & 15)) << 4) + 8 * hh,
              u32x2{pk(o0[4 * a] * inv_l, o0[4 * a + 1] * inv_l), pk(o0[4 * a + 2] * inv_l, o0[4 * a + 3] * inv_l)});
      lds_w64(L0 + TB + r31 * 1024 + (((8 * w + 4 + a) ^ (r31 & 15)) << 4) + 8 * hh,
              u32x2{pk(o1[4 * a] * inv_l, o1[4 * a + 1] * inv_l), pk(o1[4 * a + 2] * inv_l, o1[4 * a + 3] * inv_l)});
    }
    BAR();
#pragma unroll
    for (int i = 0; i < 4; ++i) {
      const u32 hd = 4 * w + i;
      const u32x4 t = lds_r128(L0 + TB + hd * 1024 + ((lane ^ (hd & 15)) << 4));
      st_sc1(ws, WS_PART + (((hd * 4 + g) * 8 + b) * 16 + s) * 256 + (lane & 15) * 16, t);
    }
    WAITV(0);
    if ((r31 >> 2) == (u32)w && lane < 32) st_sc1(ws, WS_STATS + ((r31 * 8 + b) * 16 + s) * 16, u32x4{asu(m_run), tag, asu(l_tot), tag});
    STAMP(9);
#pragma unroll
    for (int i = 0; i < 4; ++i) dma_nt(p.wo_a + wo_off + (size_t)i * 32 * 1024, wo_vo, L0 + 32768 + ((i * 8 + w) << 10));
#pragma unroll
    for (int i = 0; i < 4; ++i) dma_nt(p.wo_b + wo_off + (size_t)i * 32 * 1024, wo_vo, L0 + 98304 + ((i * 8 + w) << 10));
    float gate;
    {
      u32x4 xv, gv;
      const u32 xo = w * 1024 + lane * 16, go = lane * 16;
      LD_DF(xv, xo, p.x, 0); LD_DF(gv, go, p.gw, 0);
      PIN2(0, xv, gv);
      float d = 0.f;
#pragma unroll
      for (int e = 0; e < 4; ++e) d += blo(xv[e]) * blo(gv[e]) + bhi(xv[e]) * bhi(gv[e]);
      d += dppf<0xB1>(d); d += dppf<0x4E>(d); d += dppf<0x141>(d); d += dppf<0x128>(d);
      d = x32sum(x16sum(d));
      gate = 1.f / (1.f + ex2(-d * 1.4426950408889634f));
    }
    constexpr u32 ACT1 = SCR, ACT2 = SCR + 2048, ACT3 = SCR + 4096;
    {
      u32x4 sv4[4];
      const u32 so = WS_STATS + ((h * 8 + w) * 16 + g) * 16;
      for (int spin = 0; spin < SPIN_CAP; ++spin) {
        LD_SC1(sv4[0], so, ws, 0); LD_SC1(sv4[1], so, ws, 64); LD_SC1(sv4[2], so, ws, 128); LD_SC1(sv4[3], so, ws, 192);
        PIN4(0, sv4[0], sv4[1], sv4[2], sv4[3]);
        if (all_lanes(tag_ok(sv4[0], tag) && tag_ok(sv4[1], tag) && tag_ok(sv4[2], tag) && tag_ok(sv4[3], tag))) break;
        __builtin_amdgcn_s_sleep(1);
      }
      STAMP(10);
      u32x4 pv[4];
      const u32 po = WS_PART + ((h * 4 + j) * 8 + w) * 4096 + lane * 16;
      LD_SC1(pv[0], po, ws, 0); LD_SC1(pv[1], po, ws, 1024); LD_SC1(pv[2], po, ws, 2048); LD_SC1(pv[3], po, ws, 3072);
      float M = fmaxf(fmaxf(asf(sv4[0][0]), asf(sv4[1][0])), fmaxf(asf(sv4[2][0]), asf(sv4[3][0])));
      M = x32max(x16max(M));
      float wt[4], wsum = 0.f;
#pragma unroll
      for (int i = 0; i < 4; ++i) { wt[i] = asf(sv4[i][2]) * ex2(asf(sv4[i][0]) - M); wsum += wt[i]; }
      wsum = x32sum(x16sum(wsum));
      const float iw = 1.f / wsum;
      PIN4(0, pv[0], pv[1], pv[2], pv[3]);
      float c8[8];
#pragma unroll
      for (int e = 0; e < 8; ++e) c8[e] = 0.f;
#pragma unroll
      for (int i = 0; i < 4; ++i) {
        const float f = wt[i] * iw;
#pragma unroll
        for (int e = 0; e < 4; ++e) { c8[2 * e] += f * blo(pv[i][e]); c8[2 * e + 1] += f * bhi(pv[i][e]); }
      }
#pragma unroll
      for (int e = 0; e < 8; ++e) c8[e] = x32sum(x16sum(c8[e]));
      if (lane < 16) lds_w128(L0 + ACT1 + w * 256 + ((lane ^ (w << 1)) << 4), u32x4{pk(c8[0], c8[1]), pk(c8[2], c8[3]), pk(c8[4], c8[5]), pk(c8[6], c8[7])});
    }
    BAR();
    {
      const f32x4 c = gemv128(L0, L0 + ACT1, w, lane, f32x4{0.f, 0.f, 0.f, 0.f});
      if ((lane & 15) < 8) {
        const u32 o = WS_CTXP + ((h * 4 + j) * 8 + (lane & 7)) * 1024 + (8 * w + 2 * g) * 16;
        st_sc1(ws, o, u32x4{asu(c[0]), tag, asu(c[1]), tag});
        st_sc1(ws, o + 16, u32x4{asu(c[2]), tag, asu(c[3]), tag});
      }
    }
    STAMP(11);
    {
      u32x4 cv[4];
      const u32 co = WS_CTXP + (h * 32 + w) * 1024 + lane * 16;
      const u32 co1 = co + 8192, co2 = co + 16384, co3 = co + 24576;
      for (int spin = 0; spin < SPIN_CAP; ++spin) {
        LD_SC1(cv[0], co, ws, 0); LD_SC1(cv[1], co1, ws, 0); LD_SC1(cv[2], co2, ws, 0); LD_SC1(cv[3], co3, ws, 0);
        PIN4(0, cv[0], cv[1], cv[2], cv[3]);
        if (all_lanes(tag_ok(cv[0], tag) && tag_ok(cv[1], tag) && tag_ok(cv[2], tag) && tag_ok(cv[3], tag))) break;
        __builtin_amdgcn_s_sleep(1);
      }
      const float v0 = ((asf(cv[0][0]) + asf(cv[1][0])) + (asf(cv[2][0]) + asf(cv[3][0]))) * gate;
      const float v1 = ((asf(cv[0][2]) + asf(cv[1][2])) + (asf(cv[2][2]) + asf(cv[3][2]))) * gate;
      lds_w32(L0 + ACT2 + w * 256 + (((lane >> 2) ^ (w << 1)) << 4) + (lane & 3) * 4, pk(v0, v1));
    }
    BAR();
    f32x4 oacc = gemv128(L0 + 32768, L0 + ACT2, w, lane, f32x4{0.f, 0.f, 0.f, 0.f});
    STAMP(12);
    {
      u32x4 ov[4];
      const u32 oo = WS_OB + (h * 8 + w) * 2048 + lane * 16, os = WS_OBS + (h * 8 + w) * 32;
      for (int spin = 0; spin < SPIN_CAP; ++spin) {
        LD_SC1(ov[0], oo, ws, 0); LD_SC1(ov[1], oo, ws, 1024); LD_SC1(ov[2], os, ws, 0); LD_SC1(ov[3], os, ws, 16);
        PIN4(0, ov[0], ov[1], ov[2], ov[3]);
        if (all_lanes(tag_ok(ov[0], tag) && tag_ok(ov[1], tag) && tag_ok(ov[2], tag) && tag_ok(ov[3], tag))) break;
        __builtin_amdgcn_s_sleep(1);
      }
      const float M = fmaxf(asf(ov[2][0]), asf(ov[3][0]));
      const float e0 = ex2(asf(ov[2][0]) - M), e1 = ex2(asf(ov[3][0]) - M);
      const float f = (1.f - gate) / (e0 * asf(ov[2][2]) + e1 * asf(ov[3][2]));
      const float v0 = (e0 * asf(ov[0][0]) + e1 * asf(ov[1][0])) * f, v1 = (e0 * asf(ov[0][2]) + e1 * asf(ov[1][2])) * f;
      lds_w32(L0 + ACT3 + w * 256 + (((lane >> 2) ^ (w << 1)) << 4) + (lane & 3) * 4, pk(v0, v1));
    }
    STAMP(13);
    BAR();
    oacc = gemv128(L0 + 98304, L0 + ACT3, w, lane, oacc);
    if ((lane & 15) < 8) {
      const u32 o = WS_OUTP + ((j * 32 + (lane & 7) * 4 + (w >> 1)) * 32 + h) * 256 + ((w & 1) * 8 + 2 * g) * 16;
      st_sc1(ws, o, u32x4{asu(oacc[0]), tag, asu(oacc[1]), tag});
      st_sc1(ws, o + 16, u32x4{asu(oacc[2]), tag, asu(oacc[3]), tag});
    }
    STAMP(14);
    if (w == 0) {
      u32x4 rv[8];
      const u32 ro = WS_OUTP + (j * 32 + h) * 8192 + lane * 16, ro2 = ro + 4096;
      for (int spin = 0; spin < SPIN_CAP; ++spin) {
        LD_SC1(rv[0], ro, ws, 0); LD_SC1(rv[1], ro, ws, 1024); LD_SC1(rv[2], ro, ws, 2048); LD_SC1(rv[3], ro, ws, 3072);
        LD_SC1(rv[4], ro2, ws, 0); LD_SC1(rv[5], ro2, ws, 1024); LD_SC1(rv[6], ro2, ws, 2048); LD_SC1(rv[7], ro2, ws, 3072);
        PIN4(0, rv[0], rv[1], rv[2], rv[3]);
        PIN4(0, rv[4], rv[5], rv[6], rv[7]);
        bool ok = true;
#pragma unroll
        for (int i = 0; i < 8; ++i) ok = ok && tag_ok(rv[i], tag);
        if (all_lanes(ok)) break;
        __builtin_amdgcn_s_sleep(1);
      }
      float v0 = 0.f, v1 = 0.f;
#pragma unroll
      for (int i = 0; i < 8; ++i) { v0 += asf(rv[i][0]); v1 += asf(rv[i][2]); }
      v0 = x32sum(x16sum(v0)); v1 = x32sum(x16sum(v1));
      if (lane < 16) *(u32*)(p.out + (h >> 2) * 512 + j * 128 + (h & 3) * 32 + lane * 2) = pk(v0, v1);
      if (blk == 0 && lane == 0) __hip_atomic_store((u32*)(ws + WS_EPOCH), tag, __ATOMIC_RELAXED, __HIP_MEMORY_SCOPE_AGENT);
      STAMP(15);
      STAMP_FLUSH();
    }
  } else {
    const int kvh = xcd, b = i16 >> 1, hf = i16 & 1;
    constexpr u32 XI = SCR, QIM = SCR + 8192, QBI_ = SCR + 10240, MXB = SCR + 11264, LBB = SCR + 11520, RED = SCR + 12288;
    dma_df(p.x + w * 1024, (u32)((lane ^ (w << 1)) << 4), L0 + XI + w * 1024);
    const u32 w_vo = g * 8192 + pc_chunk;
#pragma unroll
    for (int i = 0; i < 16; ++i) {
      const int m0 = (32 * i + 4 * w + 128 * j) & 511;
      dma_nt(p.wq_a + (size_t)m0 * 8192 + h * 256, w_vo, L0 + ((i * 8 + w) << 10));
    }
    u32x4 uk[4];
    {
      const unsigned char* ub = p.wuk + (size_t)(128 * j + 16 * w) * 8192 + h * 256;
      const u32 uo = (lane & 15) * 8192 + g * 16;
      LD_NT(uk[0], uo, ub, 0); LD_NT(uk[1], uo, ub, 64); LD_NT(uk[2], uo, ub, 128); LD_NT(uk[3], uo, ub, 192);
    }
    const size_t kv_off = (((size_t)(b * 8 + kvh) * 4096 + 3072 + hf * 512) + 16 * w) * 256;
    const unsigned char* kb = p.kc + kv_off;
    const unsigned char* vb = p.vc + kv_off;
    const u32 k_vo = (lane & 15) * 256 + g * 16;
    u32x4 kf[16];
#define B_K(i) LD_NT(kf[i], k_vo, kb + (size_t)((i) >> 2) * 32768, ((i) & 3) * 64)
#define B_V(i) dma_nt(vb + (size_t)((i) >> 2) * 32768 + ((i) & 3) * 1024, v_vo[(2 * ((i) & 3) + (((i) >> 2) & 1)) & 3], \
                      L0 + w * 16384 + (((i) >> 3) * 8 + ((i) & 3) * 2 + (((i) >> 2) & 1)) * 1024)
    u32 v_vo[4];
#pragma unroll
    for (int i = 0; i < 4; ++i) v_vo[i] = g * 256 + (((lane & 15) ^ ((g << 2) | i)) << 4);
#pragma unroll
    for (int i = 0; i < DB; ++i) B_K(i);
    PIN4(DB, uk[0], uk[1], uk[2], uk[3]);
    BAR();
    STAMP(1);
    const u32 tq = (lane & 15) >> 2, tp = lane & 3, b7 = lane & 7;
    {
      f32x4 qa = {0.f, 0.f, 0.f, 0.f};
#pragma unroll
      for (int s = 0; s < 16; ++s) {
        u32x2 a[2];
#pragma unroll
        for (int t = 0; t < 2; ++t) {
          const u32 row = 8 * g + 4 * t + tq;
          a[t] = lds_tr(L0 + s * 8192 + row * 256 + (((2 * w + (tp >> 1)) ^ fsw(row)) << 4) + 8 * (tp & 1));
        }
        const u32x4 xf = lds_r128(L0 + XI + b7 * 1024 + ((((4 * s + g + 16 * j) & 63) ^ (b7 << 1)) << 4));
        qa = mfma16(cat(a[0], a[1]), xf, qa);
      }
      if ((lane & 15) < 8) lds_w64(L0 + QIM + b7 * 256 + (((2 * w + (g >> 1)) ^ (b7 << 1)) << 4) + 8 * (g & 1), u32x2{pk(qa[0], qa[1]), pk(qa[2], qa[3])});
    }
    BAR();
#pragma unroll
    for (int i = 0; i < 4; ++i) dma_nt(p.wq_b + (size_t)(128 * j + 32 * i + 4 * w) * 8192 + h * 256, w_vo, L0 + ((i * 8 + w) << 10));
    {
      f32x4 c = {0.f, 0.f, 0.f, 0.f};
#pragma unroll
      for (int s = 0; s < 4; ++s) c = mfma16(uk[s], lds_r128(L0 + QIM + b7 * 256 + (((4 * s + g) ^ (b7 << 1)) << 4)), c);
      if ((lane & 15) < 8) st_sc1(ws, WS_QABS + b7 * 65536 + h * 2048 + (32 * j + 4 * w + g) * 16, u32x4{pk(c[0] * C2, c[1] * C2), tag, pk(c[2] * C2, c[3] * C2), tag});
    }
    STAMP(2);
    constexpr int K1 = (2 * DB < 16) ? 2 * DB : 16;
#pragma unroll
    for (int i = DB; i < K1; ++i) { WAITV(DB + 3); B_K(i); }
    WAITV(K1 - DB);
    BAR();
    STAMP(3);
    {
      f32x4 c = {0.f, 0.f, 0.f, 0.f};
#pragma unroll
      for (int s = 0; s < 4; ++s) {
        u32x2 a[2];
#pragma unroll
        for (int t = 0; t < 2; ++t) {
          const u32 row = 8 * g + 4 * t + tq;
          a[t] = lds_tr(L0 + s * 8192 + row * 256 + (((2 * w + (tp >> 1)) ^ fsw(row)) << 4) + 8 * (tp & 1));
        }
        const u32x4 xf = lds_r128(L0 + XI + b7 * 1024 + (((4 * s + g + 16 * j) ^ (b7 << 1)) << 4));
        c = mfma16(cat(a[0], a[1]), xf, c);
      }
      if ((lane & 15) < 8) {
        const u32 o = WS_QB + ((b7 * 32 + h) * 4 + j) * 1024 + (8 * w + 2 * g) * 16;
        st_sc1(ws, o, u32x4{asu(c[0]), tag, asu(c[1]), tag});
        st_sc1(ws, o + 16, u32x4{asu(c[2]), tag, asu(c[3]), tag});
      }
    }
    BAR();
    STAMP(4);
#pragma unroll
    for (int i = K1; i < 16; ++i) { WAITV(DB - 1); B_K(i); }
    u32x4 qv[4];
    const u32 qo = WS_QB + (b * 32 + kvh * 4 + (w & 3)) * 4096 + lane * 16;
#define B_QLOAD() do { LD_SC1(qv[0], qo, ws, 0); LD_SC1(qv[1], qo, ws, 1024); LD_SC1(qv[2], qo, ws, 2048); LD_SC1(qv[3], qo, ws, 3072); } while (0)
    if (w < 4) B_QLOAD();
#pragma unroll
    for (int i = 0; i < DB; ++i) { if (w < 4) WAITV(DB + 3); else WAITV(DB - 1); B_V(i); }
    if (w < 4) {
      PIN4(DB, qv[0], qv[1], qv[2], qv[3]);
      STAMP(5);
      for (int spin = 0; spin < SPIN_CAP; ++spin) {
        if (all_lanes(tag_ok(qv[0], tag) && tag_ok(qv[1], tag) && tag_ok(qv[2], tag) && tag_ok(qv[3], tag))) break;
        __builtin_amdgcn_s_sleep(2);
        B_QLOAD();
        PIN4(0, qv[0], qv[1], qv[2], qv[3]);
      }
      const float v0 = (asf(qv[0][0]) + asf(qv[1][0])) + (asf(qv[2][0]) + asf(qv[3][0]));
      const float v1 = (asf(qv[0][2]) + asf(qv[1][2])) + (asf(qv[2][2]) + asf(qv[3][2]));
      lds_w32(L0 + QBI_ + w * 256 + (((lane >> 2) ^ (w << 2)) << 4) + (lane & 3) * 4, pk(v0, v1));
    }
    PIN4(DB, kf[0], kf[1], kf[2], kf[3]); PIN4(DB, kf[4], kf[5], kf[6], kf[7]);
    PIN4(DB, kf[8], kf[9], kf[10], kf[11]); PIN4(DB, kf[12], kf[13], kf[14], kf[15]);
    BAR();
    STAMP(6);
    const u32 hd = lane & 3;
    f32x4 sc[4];
    {
      u32x4 qf[4];
#pragma unroll
      for (int s = 0; s < 4; ++s) qf[s] = lds_r128(L0 + QBI_ + hd * 256 + (((4 * s + g) ^ (hd << 2)) << 4));
#pragma unroll
      for (int t = 0; t < 4; ++t) {
        sc[t] = f32x4{0.f, 0.f, 0.f, 0.f};
#pragma unroll
        for (int s = 0; s < 4; ++s) sc[t] = mfma16(kf[4 * t + s], qf[s], sc[t]);
      }
    }
    float mx = -INFINITY;
#pragma unroll
    for (int t = 0; t < 4; ++t) mx = fmaxf(mx, fmaxf(fmaxf(sc[t][0], sc[t][1]), fmaxf(sc[t][2], sc[t][3])));
    mx = x32max(x16max(mx));
    if (lane < 4) lds_w32(L0 + MXB + lane * 32 + w * 4, asu(mx));
    BAR();
    {
      const u32x4 t0 = lds_r128(L0 + MXB + hd * 32), t1 = lds_r128(L0 + MXB + hd * 32 + 16);
      mx = fmaxf(fmaxf(fmaxf(asf(t0[0]), asf(t0[1])), fmaxf(asf(t0[2]), asf(t0[3]))), fmaxf(fmaxf(asf(t1[0]), asf(t1[1])), fmaxf(asf(t1[2]), asf(t1[3]))));
    }
    const float m2 = mx * C2;
    u32x4 pf[2];
    float ls = 0.f;
#pragma unroll
    for (int t = 0; t < 4; ++t) {
      const float p0 = ex2(sc[t][0] * C2 - m2), p1 = ex2(sc[t][1] * C2 - m2), p2 = ex2(sc[t][2] * C2 - m2), p3 = ex2(sc[t][3] * C2 - m2);
      ls += (p0 + p1) + (p2 + p3);
      pf[t >> 1][2 * (t & 1)] = pk(p0, p1); pf[t >> 1][2 * (t & 1) + 1] = pk(p2, p3);
    }
    ls = x32sum(x16sum(ls));
    if (lane < 4) lds_w32(L0 + LBB + lane * 32 + w * 4, asu(ls));
    STAMP(7);
    f32x4 oa[2] = {f32x4{0.f, 0.f, 0.f, 0.f}, f32x4{0.f, 0.f, 0.f, 0.f}};
#pragma unroll
    for (int pi = 0; pi < 2; ++pi) {
      const int lo = (pi == 0) ? DB : ((8 + DB < 16) ? 8 + DB : 16), hi = (pi == 0) ? ((8 + DB < 16) ? 8 + DB : 16) : 16;
#pragma unroll
      for (int i = lo; i < hi; ++i) { WAITV(DB - 1); B_V(i); }
      u32x4 pm[4];
#pragma unroll
      for (int gq = 0; gq < 4; ++gq)
#pragma unroll
        for (int e = 0; e < 4; ++e) pm[gq][e] = (tq == (u32)gq) ? pf[pi][e] : 0u;
      SB();
      WAITV(hi - 8 * (pi + 1));
      if (pi == 1) STAMP(8);
#pragma unroll
      for (int n = 0; n < 8; ++n) {
        u32x2 a[2];
#pragma unroll
        for (int t = 0; t < 2; ++t) {
          const u32 row = 8 * g + 4 * t + tq;
          a[t] = lds_tr(L0 + w * 16384 + pi * 8192 + row * 256 + (((2 * n + (tp >> 1)) ^ fsw(row)) << 4) + 8 * (tp & 1));
        }
        oa[n >> 2] = mfma16(pm[n & 3], cat(a[0], a[1]), oa[n >> 2]);
      }
    }
#pragma unroll
    for (int u = 0; u < 2; ++u)
#pragma unroll
      for (int r = 0; r < 4; ++r) lds_w32(L0 + RED + w * 2048 + r * 512 + u * 256 + lane * 4, asu(oa[u][r]));
    BAR();
    if (w < 4) {
      float v0 = 0.f, v1 = 0.f;
#pragma unroll
      for (int i = 0; i < 8; ++i) { const u32x2 t = lds_r64(L0 + RED + i * 2048 + w * 512 + lane * 8); v0 += asf(t[0]); v1 += asf(t[1]); }
      const u32 slot = ((kvh * 4 + w) * 8 + b) * 2 + hf;
      st_sc1(ws, WS_OB + slot * 1024 + lane * 16, u32x4{asu(v0), tag, asu(v1), tag});
      const u32x4 t0 = lds_r128(L0 + LBB + w * 32), t1 = lds_r128(L0 + LBB + w * 32 + 16);
      const float lt = ((asf(t0[0]) + asf(t0[1])) + (asf(t0[2]) + asf(t0[3]))) + ((asf(t1[0]) + asf(t1[1])) + (asf(t1[2]) + asf(t1[3])));
      if (lane == w) st_sc1(ws, WS_OBS + slot * 16, u32x4{asu(m2), tag, asu(lt), tag});
    }
    STAMP(9);
    STAMP_FLUSH();
  }
}

extern "C" void kernel_launch(void* const* d_in, const int* in_sizes, int n_in, void* d_out, int out_size, void* d_ws, size_t ws_size,
                              hipStream_t stream) {
  Params p;
  p.x = (const unsigned char*)d_in[0]; p.ckv = (const unsigned char*)d_in[1]; p.wq_a = (const unsigned char*)d_in[2];
  p.wuk = (const unsigned char*)d_in[3]; p.wuv = (const unsigned char*)d_in[4]; p.wo_a = (const unsigned char*)d_in[5];
  p.kc = (const unsigned char*)d_in[7]; p.vc = (const unsigned char*)d_in[8]; p.wq_b = (const unsigned char*)d_in[9];
  p.wo_b = (const unsigned char*)d_in[10]; p.gw = (const unsigned char*)d_in[11];
  p.ws = (unsigned char*)d_ws; p.out = (unsigned short*)d_out;
  (void)hipFuncSetAttribute((const void*)fused_decode, hipFuncAttributeMaxDynamicSharedMemorySize, LDS_BYTES);
  fused_decode<<<256, 512, LDS_BYTES, stream>>>(p);
}
```

```cpp
#include <hip/hip_runtime.h>
#include <stdint.h>

typedef unsigned u32;
typedef unsigned u32x2 __attribute__((ext_vector_type(2)));
typedef unsigned u32x4 __attribute__((ext_vector_type(4)));
typedef float f32x2 __attribute__((ext_vector_type(2)));
typedef float f32x4 __attribute__((ext_vector_type(4)));
typedef float f32x16 __attribute__((ext_vector_type(16)));
typedef __bf16 bf16x2 __attribute__((ext_vector_type(2)));
typedef __bf16 bf16x8 __attribute__((ext_vector_type(8)));
typedef short s16x4 __attribute__((ext_vector_type(4)));
#define LAS __attribute__((address_space(3)))
#define DI __device__ __forceinline__
#define ROLE_OF(b) (((b) >> 3) & 1)

#define KF 0
#define DB 8
#define QPL 0
#define TLBW 1
#define TP 0
#define APRE 32
#define TR 99
#define WPOS 3
#define VQ 8
#define S2M 0
#define KSW 8
#define BLK 4
#define UKE 1
#define DF 8
#define DA 8
#define QI 28
#define QW 2
#define S2W 1
#define S2E 0
#define KP 2
#define DK 3
#define DRY 0
#define LOCK 4
#define AIS 2
#define WQE 1
#define QSL 0
#define S1W 0
#define S3W 0
#define S4W 0
#define TLBA 1
#define GAP1 16
#define GAP3 12
#define GAP4 12
#define KD 0
#define QB 1
#define PL 1
#define VF 8
#define S2B 1
#define GAPH 12
#define LATE3 0

constexpr u32 WS_EPOCH = 0;
constexpr u32 WS_HINT = 4096;
constexpr u32 WS_STATS = WS_HINT + 16384;
constexpr u32 WS_OBS = WS_STATS + 65536;
constexpr u32 WS_QABS = WS_OBS + 8192;
constexpr u32 WS_QP = WS_QABS + 524288;
constexpr u32 WS_CTXP = WS_QP + 1048576;
constexpr u32 WS_OB = WS_CTXP + 1048576;
constexpr u32 WS_QB = WS_OB + 524288;
constexpr u32 WS_OUTP = WS_QB + 1048576;
constexpr u32 WS_PART = WS_OUTP + 2097152;
constexpr u32 WS_END = WS_PART + 8388608;
constexpr int LDS_BYTES = 163840;
constexpr u32 SCR = 131072;
constexpr int SPIN_CAP = 1 << 15;
constexpr float C2 = 0.08838834764831845f * 1.4426950408889634f;

struct Params {
  const unsigned char *x, *ckv, *wq_a, *wuk, *wuv, *wo_a, *kc, *vc, *wq_b, *wo_b, *gw;
  unsigned char* ws;
  unsigned short* out;
};

#define GRD ""
#define RC "i"
#define WAITV(n) asm volatile(GRD "s_waitcnt vmcnt(%0)\n1:" ::"i"(n), [r] RC(real) : "memory", "scc")
#define BAR() asm volatile("s_waitcnt lgkmcnt(0)\n\t" GRD "s_barrier\n1:" ::[r] RC(real) : "memory", "scc")
#define OPAQUE(x) asm volatile("" : "+v"(x))
#define OPAQUE_S(x) asm volatile("" : "+s"(x))
DI u32 lane_id() { u32 l; asm volatile("v_mbcnt_lo_u32_b32 %0, -1, 0\n\tv_mbcnt_hi_u32_b32 %0, -1, %0" : "=v"(l)); __builtin_assume(l < 64); return l; }
#define SEC(n) if (real || w == (n))
#define SB() __builtin_amdgcn_sched_barrier(0)

#define DMA_(pol, sbase, voff, lds) asm volatile(GRD "s_mov_b32 m0, %2\n\ts_nop 0\n\tglobal_load_lds_dwordx4 %0, %1" pol "\n1:" \
               ::"v"((u32)(voff)), "s"(sbase), "s"((u32)(lds)), [r] RC(real) : "memory", "scc")
#define dma_nt(sbase, voff, lds) DMA_(" nt", sbase, voff, lds)
#define dma_df(sbase, voff, lds) DMA_("", sbase, voff, lds)
#define LD_(pol, dst, voff, sbase, imm) asm volatile(GRD "global_load_dwordx4 %0, %1, %2 offset:%3" pol "\n1:" : "=v"(dst) : "v"(voff), "s"(sbase), "i"(imm), [r] RC(real) : "memory", "scc")
#define LD_NT(dst, voff, sbase, imm) LD_(" nt", dst, voff, sbase, imm)
#define LD_SC1(dst, voff, sbase, imm) LD_(" sc1", dst, voff, sbase, imm)
#define LD_DF(dst, voff, sbase, imm) LD_("", dst, voff, sbase, imm)
#define LD4_SC1(d, i0, voff, sbase) asm volatile(GRD "global_load_dwordx4 %0, %4, %5 sc1\n\tglobal_load_dwordx4 %1, %4, %5 offset:1024 sc1\n\t" \
               "global_load_dwordx4 %2, %4, %5 offset:2048 sc1\n\tglobal_load_dwordx4 %3, %4, %5 offset:3072 sc1\n1:" \
               : "=&v"(d[i0]), "=&v"(d[i0 + 1]), "=&v"(d[i0 + 2]), "=&v"(d[i0 + 3]) : "v"(voff), "s"(sbase), [r] RC(real) : "memory", "scc")
#define RELOAD(v, voff, imm) do { unsigned long long e_; asm volatile("v_cmp_ne_u32 vcc, 0, %2\n\ts_and_saveexec_b64 %1, vcc\n\t" \
               "global_load_dwordx4 %0, %3, %4 offset:%5 sc1\n\ts_mov_b64 exec, %1" : "+v"(v), "=&s"(e_) : "v"(tdiff(v, tag)), "v"(voff), "s"(ws), "i"(imm) : "vcc", "scc", "memory"); } while (0)
#define RELOAD8(d, vo0, vo1) do { RELOAD(d[0], vo0, 0); RELOAD(d[1], vo0, 1024); RELOAD(d[2], vo0, 2048); RELOAD(d[3], vo0, 3072); \
               RELOAD(d[4], vo1, 0); RELOAD(d[5], vo1, 1024); RELOAD(d[6], vo1, 2048); RELOAD(d[7], vo1, 3072); } while (0)
#define dma_sc1(voff, lds) DMA_(" sc1", ws, voff, lds)
#define DMA3(o1, o2, o3, sl) do { dma_sc1(o1, sl); dma_sc1(o2, (sl) + 8192); dma_sc1(o3, (sl) + 16384); } while (0)
#define POLL3(o1, o2, o3, gap, base) do { u32 sl_ = (base) + w * 1024; const u32 sx_ = 2 * sl_ + 24576; \
      DMA3(o1, o2, o3, sl_); __builtin_amdgcn_s_sleep(gap); DMA3(o1, o2, o3, sl_ + 24576); \
      _Pragma("nounroll") for (int spin_ = 0; spin_ < SPIN_CAP; ++spin_) { \
        WAITV(3); \
        const u32x4 c0_ = lds_r128(sl_ + lane * 16), c1_ = lds_r128(sl_ + lane * 16 + 8192), c2_ = lds_r128(sl_ + lane * 16 + 16384); \
        v0 = (own0 + asf(c0_[0])) + (asf(c1_[0]) + asf(c2_[0])); v1 = (own1 + asf(c0_[2])) + (asf(c1_[2]) + asf(c2_[2])); \
        if (all_zero(tdiff(c0_, tag) | tdiff(c1_, tag) | tdiff(c2_, tag))) break; \
        DMA3(o1, o2, o3, sl_); sl_ = sx_ - sl_; } } while (0)
#define TOUCH(dst, voff, sbase) asm volatile("global_load_dword %0, %1, %2" : "=v"(dst) : "v"(voff), "s"(sbase) : "memory")
#define st_sc1(sbase, voff, ...) do { const u32x4 v_ = __VA_ARGS__; \
               asm volatile(GRD "global_store_dwordx4 %0, %1, %2 sc1\n\ts_nop 1\n1:" ::"v"((u32)(voff)), "v"(v_), "s"(sbase), [r] RC(real) : "memory", "scc"); } while (0)
#define PIN4(n, a, b, c, d) asm volatile(GRD "s_waitcnt vmcnt(%4) ; PIN %0 %1 %2 %3\n1:" : "+v"(a), "+v"(b), "+v"(c), "+v"(d) : "i"(n), [r] RC(real) : "memory", "scc")
#define PIN8(n, d, i0) asm volatile(GRD "s_waitcnt vmcnt(%8) ; PIN %0 %1 %2 %3 %4 %5 %6 %7\n1:" : "+v"(d[i0]), "+v"(d[i0 + 1]), "+v"(d[i0 + 2]), "+v"(d[i0 + 3]), \
               "+v"(d[i0 + 4]), "+v"(d[i0 + 5]), "+v"(d[i0 + 6]), "+v"(d[i0 + 7]) : "i"(n), [r] RC(real) : "memory", "scc")
#define PIN1(n, a) asm volatile(GRD "s_waitcnt vmcnt(%1) ; PIN %0\n1:" : "+v"(a) : "i"(n), [r] RC(real) : "memory", "scc")
#define PIN2(n, a, b) asm volatile(GRD "s_waitcnt vmcnt(%2) ; PIN %0 %1\n1:" : "+v"(a), "+v"(b) : "i"(n), [r] RC(real) : "memory", "scc")

DI u32x4 lds_r128(u32 a) { return *(LAS u32x4*)a; }
DI u32x2 lds_r64(u32 a) { return *(LAS u32x2*)a; }
DI void lds_w128(u32 a, u32x4 v) { *(LAS u32x4*)a = v; }
DI void lds_w64(u32 a, u32x2 v) { *(LAS u32x2*)a = v; }
DI void lds_w32(u32 a, u32 v) { *(LAS u32*)a = v; }
DI u32x2 lds_tr(u32 a) { return __builtin_bit_cast(u32x2, __builtin_amdgcn_ds_read_tr16_b64_v4i16((LAS s16x4*)a)); }
DI u32x4 cat(u32x2 a, u32x2 b) { return u32x4{a[0], a[1], b[0], b[1]}; }
DI f32x16 mfma32(u32x4 a, u32x4 b, f32x16 c) { return __builtin_amdgcn_mfma_f32_32x32x16_bf16(__builtin_bit_cast(bf16x8, a), __builtin_bit_cast(bf16x8, b), c, 0, 0, 0); }
DI f32x4 mfma16(u32x4 a, u32x4 b, f32x4 c) { return __builtin_amdgcn_mfma_f32_16x16x32_bf16(__builtin_bit_cast(bf16x8, a), __builtin_bit_cast(bf16x8, b), c, 0, 0, 0); }
DI u32 pk(float lo, float hi) { return __builtin_bit_cast(u32, __builtin_convertvector(f32x2{lo, hi}, bf16x2)); }
DI float blo(u32 v) { return __uint_as_float(v << 16); }
DI float bhi(u32 v) { return __uint_as_float(v & 0xffff0000u); }
DI float asf(u32 v) { return __uint_as_float(v); }
DI u32 asu(float v) { return __float_as_uint(v); }
DI float ex2(float v) { return __builtin_amdgcn_exp2f(v); }
DI float x32max(float v) { auto t = __builtin_amdgcn_permlane32_swap(asu(v), asu(v), false, false); return fmaxf(asf(t[0]), asf(t[1])); }
DI float x32sum(float v) { auto t = __builtin_amdgcn_permlane32_swap(asu(v), asu(v), false, false); return asf(t[0]) + asf(t[1]); }
DI float x16max(float v) { auto t = __builtin_amdgcn_permlane16_swap(asu(v), asu(v), false, false); return fmaxf(asf(t[0]), asf(t[1])); }
DI float x16sum(float v) { auto t = __builtin_amdgcn_permlane16_swap(asu(v), asu(v), false, false); return asf(t[0]) + asf(t[1]); }
template <int CTRL> DI float dppf(float v) { return __builtin_bit_cast(float, __builtin_amdgcn_mov_dpp(__builtin_bit_cast(int, v), CTRL, 0xf, 0xf, true)); }
DI u32 tdiff(u32x4 v, u32 tag) { return (v[1] ^ tag) | (v[3] ^ tag); }
#define all_zero(d) (__builtin_amdgcn_ballot_w64((d) != 0u) == 0ull || !real)
DI float max4(u32x4 t) { return fmaxf(fmaxf(asf(t[0]), asf(t[1])), fmaxf(asf(t[2]), asf(t[3]))); }
DI float sum4(u32x4 t) { return (asf(t[0]) + asf(t[1])) + (asf(t[2]) + asf(t[3])); }
DI u32 bperm(u32 src_lane4, u32 v) { return (u32)__builtin_amdgcn_ds_bpermute((int)src_lane4, (int)v); }
DI u32 fsw(u32 row) { return ((row & 3) << 2) | ((row >> 2) & 3); }

#define STAMP(i)
#define STAMP_FLUSH()

template <int N> DI void wfrag(u32x4 (&a)[N], u32 wimg, u32 col2, u32 lane) {
  const u32 row = 8 * (lane >> 4) + ((lane & 15) >> 2), pp = lane & 3;
  const u32 a0 = wimg + row * 256 + (((col2 + (pp >> 1)) ^ fsw(row)) << 4) + 8 * (pp & 1), a1 = (a0 ^ 16) + 1024;
#pragma unroll
  for (int s = 0; s < N; ++s) a[s] = cat(lds_tr(a0 + s * 8192), lds_tr(a1 + s * 8192));
}
DI void afrag(u32x4 (&x)[4], u32 act, u32 lane) {
  const u32 b = lane & 7, a0 = act + b * 256 + (((lane >> 4) ^ (b << 1)) << 4);
#pragma unroll
  for (int s = 0; s < 4; ++s) x[s] = lds_r128(a0 ^ (s << 6));
}
DI f32x4 mm4(const u32x4 (&a)[4], const u32x4 (&x)[4], f32x4 acc) {
#pragma unroll
  for (int s = 0; s < 4; ++s) acc = mfma16(a[s], x[s], acc);
  return acc;
}

DI float gate_of(u32x4 xv, u32x4 gv) {
  float d = 0.f;
#pragma unroll
  for (int e = 0; e < 4; ++e) d += blo(xv[e]) * blo(gv[e]) + bhi(xv[e]) * bhi(gv[e]);
  d += dppf<0xB1>(d); d += dppf<0x4E>(d); d += dppf<0x141>(d); d += dppf<0x128>(d);
  d = x32sum(x16sum(d));
  return 1.f / (1.f + ex2(-d * 1.4426950408889634f));
}

__global__ __launch_bounds__(512, 2) void fused_decode(Params p) {
  extern __shared__ __attribute__((aligned(16))) unsigned char smem[];
  const int blk0 = blockIdx.x;
  const int w0 = __builtin_amdgcn_readfirstlane(threadIdx.x >> 6);
#define IDS() const int xcd = blk & 7, i16 = blk >> 4, h = (xcd >> 2) * 16 + i16, j = xcd & 3; (void)h; (void)j
#define PASS_LOOP
#define PASS_HEAD() constexpr int real = 1; const int blk = blk0, w = w0; IDS(); unsigned char* const ws = p.ws
  const u32 L0 = (u32)(size_t)(LAS unsigned char*)smem;
  STAMP(0);
#define TAG_REQ() u32 tag; asm volatile("s_load_dword %0, %1, 0x0 ; TAGREQ" : "=s"(tag) : "s"(p.ws) : "memory")
#define TAG_GET() do { asm volatile("s_waitcnt lgkmcnt(0) ; TAGGET %0" : "+s"(tag) :: "memory"); tag = tag * 1664525u + 1013904223u; } while (0)
  int npass = 2;
  asm volatile("" : "+s"(npass));

  if (ROLE_OF(blk0) == 0) {
#define A_IDS() const int b = xcd, s = i16; const unsigned char* cb = p.ckv + ((size_t)(b * 4096 + s * 256 + w)) * 1024; \
                const u32 f0 = ((w & 3) << 2) | ((w >> 2) & 3), f1 = ((w & 3) << 2) | ((2 + (w >> 2)) & 3)
    {
      const int blk = blk0, w = w0; constexpr int real = 1;
      IDS(); A_IDS();
      const u32 l = lane_id();
      __builtin_amdgcn_s_sleep(16);
#pragma unroll
      for (int i = 0; i < DF; ++i) dma_nt(cb + (size_t)i * 8192, (l ^ ((i & 1) ? f1 : f0)) << 4, L0 + ((i * 8 + w) << 10));
    }
    TAG_REQ();
    PASS_LOOP {
    PASS_HEAD();
    A_IDS();
    if (real) STAMP(1);
    const u32 lane = lane_id(), g = lane >> 4;
    const u32 pc_chunk = ((lane & 15) ^ ((g << 2) | (w & 3))) << 4;
    u32 vo[2];
    vo[0] = (lane ^ f0) << 4;
    vo[1] = (lane ^ f1) << 4;
    const u32 l16 = lane * 16;
    const u32 wo_vo = g * 1024 + pc_chunk;
    const size_t wo_off = (size_t)(h * 128 + 4 * w) * 1024 + j * 256;
    u32x4 rb[16], wb[8];
    const unsigned char* uvb = p.wuv + (size_t)(128 * j + 4 * w) * 8192 + h * 256;
    const u32 uv_vo = g * 8192 + pc_chunk;
#define A_ROW(i) do { if ((i) < 16) dma_nt(cb + (size_t)(i) * 8192, vo[(i) & 1], L0 + (((i) * 8 + w) << 10)); \
                      else if ((i) < 32) LD_NT(rb[(i) & 15], l16, cb + (size_t)(i) * 8192, 0); \
                      else if ((i) < 36) LD_NT(wb[(i) & 3], uv_vo, uvb + (size_t)((i) & 3) * 32 * 8192, 0); \
                      else if ((i) < 40) LD_NT(wb[4 + ((i) & 3)], wo_vo, p.wo_a + wo_off + (size_t)((i) & 3) * 32768, 0); } while (0)
#define A_PT(n) do { _Pragma("unroll") for (int i_ = APRE + (n) * AIS; i_ < ((n) == 3 ? 32 : APRE + ((n) + 1) * AIS); ++i_) if (i_ < 32) A_ROW(i_); } while (0)
    u32x4 qv[8];
    const u32 q_vo = WS_QABS + b * 65536 + w * 8192 + l16, q_vo2 = q_vo + 4096;
#define A_QLOAD() do { LD4_SC1(qv, 0, q_vo, ws); LD4_SC1(qv, 4, q_vo2, ws); } while (0)
    float gate;
    u32 tl = 0;
    u32x4 qf[16];
    const int kg = w >> 1;
    const u32 hh = lane >> 5, r31 = lane & 31, r15 = lane & 15, hq = (w & 1) * 16 + r15;
    const u32 fr_off = g * 256 + (fsw(r15) << 4);
    SEC(0) {
    {
      u32x4 xv, gv;
      LD_DF(xv, l16, p.x + w * 1024, 0); LD_DF(gv, l16, p.gw, 0);
#pragma unroll
      for (int i = DF; i < DF + DA; ++i) { WAITV(DA + 1); A_ROW(i); }
      PIN2(DA, xv, gv);
      TAG_GET();
      gate = gate_of(xv, gv);
    }
#pragma unroll
    for (int i = DF + DA; i < APRE; ++i) {
      if (i == TR) TOUCH(tl, WS_QABS + b * 65536 + w * 8192 + (lane & 1) * 4096, ws);
      if (i == QI) { WAITV(QW); if (QB) BAR(); STAMP(1); A_QLOAD(); if (QB) BAR(); }
      if (i < QI) WAITV(DA - 1 + (i >= TR && i - DA < TR));
      if (i < QI && LOCK && i % (LOCK ? LOCK : 1) == 0) BAR();
      A_ROW(i);
    }
    if (QI >= APRE) { WAITV(QW); STAMP(1); A_QLOAD(); }
    PIN8(QI >= APRE ? 0 : APRE - QI, qv, 0);
    asm volatile("; PIN %0" : "+v"(tl));
    STAMP(2);
#pragma nounroll
    for (int spin = 0; spin < SPIN_CAP; ++spin) {
      u32 d = 0;
#pragma unroll
      for (int i = 0; i < 8; ++i) {
        const u32 hd = 4 * w + (i >> 1), ch = 32 * (i & 1) + (lane >> 1);
        lds_w64(L0 + SCR + hd * 1024 + ((ch ^ fsw(hd)) << 4) + 8 * (lane & 1), u32x2{qv[i][0], qv[i][2]});
        d |= tdiff(qv[i], tag);
      }
      if (all_zero(d)) break;
      asm volatile("s_waitcnt lgkmcnt(0)" ::: "memory");
      RELOAD8(qv, q_vo, q_vo2);
      PIN8(0, qv, 0);
    }
    STAMP(3);
    A_PT(0);
    BAR();
    A_PT(1);
#pragma unroll
    for (int k = 0; k < 16; ++k) qf[k] = lds_r128(L0 + SCR + hq * 1024 + (fr_off ^ (k << 4)));
    }
    const u32 qk_base = L0 + (kg * 16 + r15) * 1024 + fr_off;
    const u32 tq = (lane & 15) >> 2, tp = lane & 3, tb = (lane >> 4) & 1;
    const u32 pv_base = L0 + (8 * hh + tq) * 1024 + (((8 * w + 2 * tb + (tp >> 1)) ^ fsw(8 * hh + tq)) << 4) + 8 * (tp & 1);
    f32x16 o0, o1;
#pragma unroll
    for (int i = 0; i < 16; ++i) { o0[i] = 0.f; o1[i] = 0.f; }
    float m_q = -INFINITY, m_o = -INFINITY, l_run = 0.f, l_tot = 0.f;
    constexpr u32 PB = SCR, MXB = SCR + 8192, LB = SCR + 8704, ACT1 = SCR + 10240, ACT2 = SCR + 12288, ACT3 = SCR + 14336, GT = SCR + 16384;
#define A_QK(dst, half) do { u32 qa = qk_base + (half); OPAQUE(qa); u32x4 af[16]; f32x4 s1_ = {0.f, 0.f, 0.f, 0.f}; dst = s1_; \
      _Pragma("unroll") for (int kk = 0; kk < 8; ++kk) af[kk] = lds_r128(qa ^ (kk << 4)); \
      SB(); \
      _Pragma("unroll") for (int kk = 0; kk < 16; kk += 2) { \
        dst = mfma16(af[kk], qf[kk], dst); s1_ = mfma16(af[kk + 1], qf[kk + 1], s1_); \
        if (kk < 8) { af[kk + 8] = lds_r128(qa ^ ((kk + 8) << 4)); af[kk + 9] = lds_r128(qa ^ ((kk + 9) << 4)); SB(); } \
      } \
      dst += s1_; } while (0)
#define A_VF(va, ks, ci) cat(lds_tr(((va) ^ ((ci) << 6)) + (ks) * 16384), lds_tr(((va) ^ ((ci) << 6) ^ 16) + (ks) * 16384 + 4096))
#define A_PUT(i) lds_w128(L0 + (((i) * 8 + w) << 10) + vo[(i) & 1], rb[i])
#pragma unroll
    for (int u = 0; u < 2; ++u) {
      STAMP(4 + 2 * u);
      if (u == 1 && TP) TOUCH(tl, WS_CTXP + (lane < 13 ? lane : 0u) * 1048576 + w * 128, ws);
      f32x4 sa, sb;
      A_QK(sa, 0);
      if (u == 1) {
        if (LATE3) {
          PIN8(8, rb, 8);
#pragma unroll
          for (int i = 8; i < 16; ++i) A_PUT(i);
        }
        if (WPOS == 3) { A_ROW(33); A_ROW(37); }
        BAR();
      }
      A_QK(sb, 65536);
      SB();
      u32 va = pv_base;
      OPAQUE(va);
      u32x4 vf[4][2];
#pragma unroll
      for (int ks = 0; ks < 4; ++ks) { vf[ks][0] = A_VF(va, ks, 0); vf[ks][1] = A_VF(va, ks, 1); }
      SB();
      if (u == 0) BAR();
      const float mx = x32max(x16max(fmaxf(fmaxf(fmaxf(sa[0], sa[1]), fmaxf(sa[2], sa[3])), fmaxf(fmaxf(sb[0], sb[1]), fmaxf(sb[2], sb[3])))));
      if (lane < 16) lds_w32(L0 + MXB + hq * 16 + kg * 4, asu(mx));
      BAR();
      const u32x4 mxq = lds_r128(L0 + MXB + hq * 16), mxo = lds_r128(L0 + MXB + r31 * 16);
      SB();
      if (u == 0) A_PT(2); else if (WPOS == 1) { _Pragma("unroll") for (int i = 32; i < 40; ++i) A_ROW(i); } else if (WPOS == 3) { A_ROW(34); A_ROW(38); }
      const float mq_new = fmaxf(m_q, max4(mxq)), mo_new = fmaxf(m_o, max4(mxo));
      const float alpha_q = ex2(m_q - mq_new), alpha_o = ex2(m_o - mo_new);
      m_q = mq_new; m_o = mo_new;
      float pa[4], pb[4];
#pragma unroll
      for (int e = 0; e < 4; ++e) { pa[e] = ex2(sa[e] - mq_new); pb[e] = ex2(sb[e] - mq_new); }
      const u32 pw = L0 + PB + kg * 1024 + (hq + 32 * hh) * 16 + tb * 8;
      lds_w64(pw, u32x2{pk(pa[0], pa[1]), pk(pa[2], pa[3])});
      lds_w64(pw + 4096, u32x2{pk(pb[0], pb[1]), pk(pb[2], pb[3])});
      l_run = l_run * alpha_q + (((pa[0] + pa[1]) + (pa[2] + pa[3])) + ((pb[0] + pb[1]) + (pb[2] + pb[3])));
      if (u == 1) {
        const float lt = x32sum(x16sum(l_run));
        if (lane < 16) lds_w32(L0 + LB + hq * 16 + kg * 4, asu(lt));
        SB();
#pragma unroll
        for (int i = 0; i < 16; ++i) { o0[i] *= alpha_o; o1[i] *= alpha_o; }
      }
      SB();
      if (u == 0) { A_PT(3); PIN8(8, rb, 0); } else if (WPOS == 3) { A_ROW(35); A_ROW(39); }
      BAR();
      if (u) STAMP(7);
      u32x4 pf[8], vg[4][2];
#pragma unroll
      for (int ks = 0; ks < 4; ++ks) pf[ks] = lds_r128(L0 + PB + ks * 1024 + l16);
      u32x4 lsum;
      if (u == 1) lsum = lds_r128(L0 + LB + r31 * 16);
      va += 65536;
      OPAQUE(va);
      SB();
#pragma unroll
      for (int ks = 0; ks < 4; ++ks) {
        o0 = mfma32(vf[ks][0], pf[ks], o0);
        if (u == 0) A_PUT(2 * ks); else if (WPOS == 2) A_ROW(32 + 2 * ks);
        vg[ks][0] = A_VF(va, ks, 0);
        SB();
        o1 = mfma32(vf[ks][1], pf[ks], o1);
        if (u == 0) A_PUT(2 * ks + 1); else if (WPOS == 2) A_ROW(33 + 2 * ks);
        vg[ks][1] = A_VF(va, ks, 1);
        pf[4 + ks] = lds_r128(L0 + PB + 4096 + ks * 1024 + l16);
        if (u == 1 && ks == 0) {
          l_tot = sum4(lsum);
          if ((lane >> 2) == (u32)w) st_sc1(ws, WS_STATS + ((r31 * 8 + b) * 16 + s) * 16, u32x4{asu(m_o), tag, asu(l_tot), tag});
        }
        SB();
      }
      if (u == 0) {
        BAR();
        if (!LATE3) PIN8(0, rb, 8);
      }
#pragma unroll
      for (int ks = 0; ks < 4; ++ks) {
        o0 = mfma32(vg[ks][0], pf[4 + ks], o0);
        if (u == 0) { if (!LATE3) A_PUT(8 + 2 * ks); if (WPOS == 0) A_ROW(32 + 2 * ks); else if (WPOS == 3 && ks < 2) A_ROW(32 + 4 * ks); SB(); }
        o1 = mfma32(vg[ks][1], pf[4 + ks], o1);
        if (u == 0) { if (!LATE3) A_PUT(9 + 2 * ks); if (WPOS == 0) A_ROW(33 + 2 * ks); SB(); }
      }
    }
    SEC(5) {
    STAMP(8);
    u32x4 sv;
    const u32 so = WS_STATS + ((h * 8 + w) * 16 + (r15 ^ hh)) * 16;
    {
      const float inv_l = 1.f / l_tot;
      u32 tw = L0 + 65536 + r31 * 1024 + (w >> 1) * 256 + (((w ^ (r31 >> 1)) & 1) << 7) + ((hh ^ r15) << 3);
      if (!real) tw = L0 + SCR;
#pragma unroll
      for (int a = 0; a < 4; ++a) {
        lds_w64(tw ^ (a << 4), u32x2{pk(o0[4 * a] * inv_l, o0[4 * a + 1] * inv_l), pk(o0[4 * a + 2] * inv_l, o0[4 * a + 3] * inv_l)});
        lds_w64(tw ^ ((4 + a) << 4), u32x2{pk(o1[4 * a] * inv_l, o1[4 * a + 1] * inv_l), pk(o1[4 * a + 2] * inv_l, o1[4 * a + 3] * inv_l)});
      }
      const u32 h4 = 2 * tb + hh;
      const u32 po = WS_PART + (PL ? b * 1048576 + ((h4 * 4 + (w >> 1)) * 16 + s) * 512 : (((h4 * 4 + (w >> 1)) * 8 + b) * 16 + s) * 512) + (w & 1) * 256 + r15 * 16;
      u32x2 t[8];
#pragma unroll
      for (int i = 0; i < 8; ++i) {
        const u32 hd = 4 * i + h4;
        t[i] = lds_r64(L0 + 65536 + hd * 1024 + (w >> 1) * 256 + (((w ^ tb) & 1) << 7) + ((r15 ^ (hd & 15)) << 3));
      }
      SB();
      if (S2E) LD_SC1(sv, so, ws, 0);
#pragma unroll
      for (int i = 0; i < 8; ++i) st_sc1(ws + (size_t)i * (PL ? 131072 : 1048576), po, u32x4{t[i][0], tag, t[i][1], tag});
    }
    STAMP(9);
    PIN8(8, wb, 0);
    asm volatile("; PIN %0" : "+v"(tl));
#pragma unroll
    for (int i = 0; i < 8; ++i) lds_w128(L0 + ((i * 8 + w) << 10) + l16, wb[i]);
    {
      u32x4 pv[8];
      const u32 po = WS_PART + (PL ? w * 128 + h * 4 + j : (h * 4 + j) * 8 + w) * 8192 + l16, po2 = po + 4096;
      if (S2W) WAITV(0);
      if (S2B) BAR();
      if (!S2E) LD_SC1(sv, so, ws, 0);
      if (S2M) { LD4_SC1(pv, 0, po, ws); LD4_SC1(pv, 4, po2, ws); }
      PIN1(S2M || S2E ? 8 : 0, sv);
      for (int spin = 0; spin < SPIN_CAP; ++spin) {
        if (all_zero(tdiff(sv, tag))) break;
        RELOAD(sv, so, 0);
        PIN1(0, sv);
      }
      STAMP(10);
      if (!S2M) { LD4_SC1(pv, 0, po, ws); LD4_SC1(pv, 4, po2, ws); }
      float M = asf(sv[0]);
      M = fmaxf(M, dppf<0xB1>(M)); M = fmaxf(M, dppf<0x4E>(M)); M = fmaxf(M, dppf<0x141>(M)); M = fmaxf(M, dppf<0x128>(M));
      float wt = asf(sv[2]) * ex2(asf(sv[0]) - M), wsum = wt;
      wsum += dppf<0xB1>(wsum); wsum += dppf<0x4E>(wsum); wsum += dppf<0x141>(wsum); wsum += dppf<0x128>(wsum);
      wt *= 1.f / wsum;
      const float f[8] = {dppf<0x150>(wt), dppf<0x152>(wt), dppf<0x154>(wt), dppf<0x156>(wt), dppf<0x158>(wt), dppf<0x15A>(wt), dppf<0x15C>(wt), dppf<0x15E>(wt)};
      PIN8(0, pv, 0);
      for (int spin = 0; spin < SPIN_CAP; ++spin) {
        u32 d = 0;
#pragma unroll
        for (int i = 0; i < 8; ++i) d |= tdiff(pv[i], tag);
        if (all_zero(d)) break;
        RELOAD8(pv, po, po2);
        PIN8(0, pv, 0);
      }
      float c4[4] = {0.f, 0.f, 0.f, 0.f};
#pragma unroll
      for (int i = 0; i < 8; ++i) {
        c4[0] += f[i] * blo(pv[i][0]); c4[1] += f[i] * bhi(pv[i][0]); c4[2] += f[i] * blo(pv[i][2]); c4[3] += f[i] * bhi(pv[i][2]);
      }
#pragma unroll
      for (int e = 0; e < 4; ++e) c4[e] = x32sum(c4[e]);
      if (lane < 32) lds_w64(L0 + ACT1 + w * 256 + (((lane >> 1) ^ (w << 1)) << 4) + (lane & 1) * 8, u32x2{pk(c4[0], c4[1]), pk(c4[2], c4[3])});
      if (lane == 0) lds_w32(L0 + GT + w * 4, asu(gate));
    }
    }
    const bool dup = (lane & 8) != 0;
    u32x4 w2[4];
    STAMP(11);
    BAR();
    float own0, own1;
    {
      u32x4 wa[4], xa[4];
      afrag(xa, L0 + ACT1, lane); wfrag<4>(wa, L0, 2 * w, lane);
      SB();
      const f32x4 c = mm4(wa, xa, f32x4{0.f, 0.f, 0.f, 0.f});
      own0 = dup ? c[2] : c[0]; own1 = dup ? c[3] : c[1];
      st_sc1(ws, WS_CTXP + ((h * 4 + j) * 8 + w) * 1024 + l16, u32x4{asu(own0), tag, asu(own1), tag});
      if (blk == 0 && w == 7 && lane == 0) asm volatile("global_store_dword %0, %1, %2 sc1\n\ts_nop 1" :: "v"(WS_EPOCH), "v"(tag), "s"(ws) : "memory");
    }
    STAMP(12);
    wfrag<4>(w2, L0 + 32768, 2 * w, lane);
    SB();
    {
      const float gate_b = asf(*(LAS u32*)(L0 + GT + (lane & 7) * 4));
      const u32 co = WS_CTXP + (h * 32 + w) * 1024 + l16, b7 = lane & 7;
      const u32 co1 = co + (j ^ 1) * 8192, co2 = co + (j ^ 2) * 8192, co3 = co + (j ^ 3) * 8192;
      float v0 = 0.f, v1 = 0.f;
      POLL3(co1, co2, co3, GAP3, L0 + 65536);
      lds_w32(L0 + ACT2 + b7 * 256 + (((2 * w + (g >> 1)) ^ (b7 << 1)) << 4) + (g & 1) * 8 + ((lane >> 1) & 4), pk(v0 * gate_b, v1 * gate_b));
    }
    STAMP(13);
    BAR();
    {
      u32x4 x2[4];
      afrag(x2, L0 + ACT2, lane);
      SB();
      const f32x4 oacc = mm4(w2, x2, f32x4{0.f, 0.f, 0.f, 0.f});
      st_sc1(ws, WS_OUTP + ((j * 32 + 4 * w + g) * 64 + 2 * h) * 256 + r15 * 16, u32x4{asu(dup ? oacc[2] : oacc[0]), tag, asu(dup ? oacc[3] : oacc[1]), tag});
    }
    STAMP(14);
    {
      const u32 ro = WS_OUTP + ((j ^ 1) * 32 + h) * 16384 + w * 2048 + l16, ro2 = ro + 1024;
      float v0 = 0.f, v1 = 0.f;
      u32 sl = L0 + w * 2048;
      const u32 sx = 2 * sl + 16384;
      dma_sc1(ro, sl); dma_sc1(ro2, sl + 1024);
      __builtin_amdgcn_s_sleep(GAP4);
      dma_sc1(ro, sl + 16384); dma_sc1(ro2, sl + 17408);
#pragma nounroll
      for (int spin = 0; spin < SPIN_CAP; ++spin) {
        WAITV(2);
        const u32x4 c0 = lds_r128(sl + l16), c1 = lds_r128(sl + l16 + 1024);
        v0 = asf(c0[0]) + asf(c1[0]); v1 = asf(c0[2]) + asf(c1[2]);
        if (all_zero(tdiff(c0, tag) | tdiff(c1, tag))) break;
        dma_sc1(ro, sl); dma_sc1(ro2, sl + 1024);
        sl = sx - sl;
      }
      v0 = x32sum(x16sum(v0)); v1 = x32sum(x16sum(v1));
      if (lane < 16) lds_w64(L0 + ACT3 + w * 128 + lane * 8, u32x2{asu(v0), asu(v1)});
      BAR();
      if (w == 7) {
        const u32x2 t0 = lds_r64(L0 + ACT3 + lane * 8), t1 = lds_r64(L0 + ACT3 + 512 + lane * 8);
        v0 = x32sum(x16sum(asf(t0[0]) + asf(t1[0]))); v1 = x32sum(x16sum(asf(t0[1]) + asf(t1[1])));
        if (lane < 16) *(u32*)(p.out + (lane & 7) * 512 + (j ^ 1) * 128 + h * 4 + ((lane >> 2) & 2)) = pk(v0, v1);
      }
      WAITV(0);
    }
    {
    STAMP(5);
    }
    }
    STAMP_FLUSH();
  } else {
    constexpr u32 QIM = SCR + 16384, QBI_ = SCR + 18432, MXB = SCR + 19456, LBB = SCR + 19712, RED = SCR;
    PASS_LOOP {
    PASS_HEAD();
    const int kvh = xcd, b = i16 >> 1, hf = i16 & 1;
    const u32 lane = lane_id(), g = lane >> 4;
    const u32 tq = (lane & 15) >> 2, b7 = lane & 7;
    u32x4 xf[4];
    const u32 w_vo = g * 8192 + (((lane & 15) ^ ((g << 2) | (w & 3))) << 4);
    const size_t wrow = (size_t)(128 * j + 4 * w) * 8192 + h * 256;
    const unsigned char* ub = p.wuk + (size_t)(128 * j + 16 * w) * 8192 + h * 256;
    const u32 uo = g * 8192 + (((lane & 15) ^ g) << 4);
#define B_UK() do { _Pragma("unroll") for (int i_ = 0; i_ < 4; ++i_) dma_nt(ub + (size_t)i_ * 4 * 8192, uo ^ (i_ << 6), L0 + 65536 + w * 4096 + i_ * 1024); } while (0)
#define B_WQ() do { _Pragma("unroll") for (int i_ = 0; i_ < 4; ++i_) dma_nt(p.wq_b + wrow + (size_t)i_ * 32 * 8192, w_vo, L0 + 32768 + ((i_ * 8 + w) << 10)); } while (0)
    const size_t kv_off = (((size_t)(b * 8 + kvh) * 4096 + 3072 + hf * 512) + 16 * w) * 256;
    const unsigned char* kb = p.kc + kv_off;
    const unsigned char* vb = p.vc + kv_off;
    u32 tl;
    const int h2 = h ^ 16;
    const unsigned char* wob = p.wo_b + (size_t)(h2 * 128 + 4 * w) * 1024 + j * 256;
    {
#pragma unroll
      for (int i = 0; i < 4; ++i) dma_nt(p.wq_a + wrow + (size_t)i * 32 * 8192, w_vo, L0 + ((i * 8 + w) << 10));
      const u32 xo = b7 * 1024 + g * 16;
      LD_DF(xf[0], xo, p.x + j * 256, 0); LD_DF(xf[1], xo, p.x + j * 256, 64); LD_DF(xf[2], xo, p.x + j * 256, 128); LD_DF(xf[3], xo, p.x + j * 256, 192);
      if (UKE) B_UK();
    }
    TAG_REQ();
    {
      if (TLBW) TOUCH(tl, lane < 2 ? WS_QABS + lane * 520192 : WS_QP + h * 32768, ws);
    }
    const u32 k_vo = (lane & 3) * 256 + tq * 64 + g * 16;
    u32x4 kf[16];
#define B_K(i) LD_NT(kf[i], k_vo, kb + (size_t)((i) >> 2) * 32768, ((i) & 3) * 1024)
#define B_V(i) dma_nt(vb + (size_t)((i) >> 2) * 32768 + ((i) & 3) * 1024, v_vo[(2 * ((i) & 3) + (((i) >> 2) & 1)) & 3], \
                      L0 + w * 16384 + (((i) >> 3) * 8 + ((i) & 3) * 2 + (((i) >> 2) & 1)) * 1024)
    constexpr int V1 = VQ + DB;
    u32 v_vo[4];
#pragma unroll
    for (int i = 0; i < 4; ++i) v_vo[i] = g * 256 + (((lane & 15) ^ ((g << 2) | i)) << 4);
    const bool dup = (lane & 8) != 0;
    PIN4((UKE ? 4 : 0) + (TLBW ? 1 : 0), xf[0], xf[1], xf[2], xf[3]);
    if (!UKE) B_UK();
    TAG_GET();
    BAR();
    STAMP(1);
    float own0, own1;
    {
      u32x4 wa[4];
      wfrag<4>(wa, L0, 2 * w, lane);
      SB();
      const f32x4 c = mm4(wa, xf, f32x4{0.f, 0.f, 0.f, 0.f});
      own0 = dup ? c[2] : c[0]; own1 = dup ? c[3] : c[1];
      st_sc1(ws, WS_QP + ((h * 4 + j) * 8 + w) * 1024 + lane * 16, u32x4{asu(own0), tag, asu(own1), tag});
    }
    STAMP(2);
    if (S1W) { WAITV(0); STAMP(12); }
#pragma unroll
    for (int i = 0; i < KF; ++i) B_K(i);
    if (QSL) __builtin_amdgcn_s_sleep(QSL);
    {
      const u32 co = WS_QP + (h * 32 + w) * 1024 + lane * 16, co1 = co + (j ^ 1) * 8192, co2 = co + (j ^ 2) * 8192, co3 = co + (j ^ 3) * 8192;
      float v0 = 0.f, v1 = 0.f;
      POLL3(co1, co2, co3, GAP1, L0 + 98304);
      STAMP(10);
      if (TLBW) asm volatile("; PIN %0" : "+v"(tl));
      B_WQ();
      lds_w32(L0 + QIM + b7 * 256 + (((2 * w + (g >> 1)) ^ (b7 << 1)) << 4) + (g & 1) * 8 + ((lane >> 1) & 4), pk(v0, v1));
    }
    u32x4 uk[4];
#pragma unroll
    for (int s = 0; s < 4; ++s) uk[s] = lds_r128(L0 + 65536 + w * 4096 + (lane & 15) * 256 + (((4 * s + g) ^ (lane & 15)) << 4));
    BAR();
    STAMP(3);
    {
      u32x4 xa[4];
      afrag(xa, L0 + QIM, lane);
      SB();
      const f32x4 c = mm4(uk, xa, f32x4{0.f, 0.f, 0.f, 0.f});
      const u32 src = ((lane & 3) * 16 + ((lane >> 2) & 7)) * 4;
      const u32 d0 = bperm(src, pk(c[0] * C2, c[1] * C2)), d1 = bperm(src, pk(c[2] * C2, c[3] * C2));
      if (lane < 32) st_sc1(ws, WS_QABS + (lane >> 2) * 65536 + h * 2048 + (32 * j + 4 * w + (lane & 3)) * 16, u32x4{d0, tag, d1, tag});
    }
    STAMP(11);
    BAR();
    if (KD) __builtin_amdgcn_s_sleep(KD);
#pragma unroll
    for (int i = KF; i < KF + KP; ++i) B_K(i);
    WAITV(KP + 1);
    BAR();
    {
      u32x4 wa[4];
      wfrag<4>(wa, L0 + 32768, 2 * w, lane);
      SB();
      const f32x4 c = mm4(wa, xf, f32x4{0.f, 0.f, 0.f, 0.f});
      const u32 src = ((lane & 7) * 8 + (lane >> 3)) * 4;
      const u32 d0 = bperm(src, asu(dup ? c[2] : c[0])), d1 = bperm(src, asu(dup ? c[3] : c[1]));
      st_sc1(ws, WS_QB + (((lane >> 3) * 32 + h) * 4 + j) * 1024 + (8 * w + (lane & 7)) * 16, u32x4{d0, tag, d1, tag});
    }
    BAR();
    SEC(3) {
    STAMP(4);
#pragma unroll
    for (int n = 0; n < VF + 16 - KF - KP; ++n) { WAITV((n < KSW ? DK : DB) - 1); if (BLK && n % (BLK ? BLK : 1) == 0) BAR(); if (n < VF) B_V(n); else B_K(n - VF + KF + KP); }
    u32x4 qv[2];
    const u32 qo = WS_QB + (b * 32 + kvh * 4 + (w & 3)) * 4096 + (lane >> 5) * 1024 + ((w >> 2) * 32 + (lane & 31)) * 16;
#define B_QLOAD() do { LD_SC1(qv[0], qo, ws, 0); LD_SC1(qv[1], qo, ws, 2048); } while (0)
#pragma unroll
    for (int i = VF; i < V1; ++i) {
      if (i == VQ) B_QLOAD();
      if (i >= VQ) WAITV(DB + 1); else WAITV(DB - 1);
      B_V(i);
    }
    PIN2(DB, qv[0], qv[1]);
    STAMP(5);
    for (int spin = 0; spin < SPIN_CAP; ++spin) {
      if (all_zero(tdiff(qv[0], tag) | tdiff(qv[1], tag))) break;
      RELOAD(qv[0], qo, 0); RELOAD(qv[1], qo, 2048);
      PIN2(0, qv[0], qv[1]);
    }
    {
      const float v0 = x32sum(asf(qv[0][0]) + asf(qv[1][0])), v1 = x32sum(asf(qv[0][2]) + asf(qv[1][2]));
      const u32 un = (w >> 2) * 32 + (lane & 31);
      if (lane < 32) lds_w32(L0 + QBI_ + (w & 3) * 256 + (((un >> 2) ^ ((w & 3) << 2)) << 4) + (un & 3) * 4, pk(v0, v1));
    }
    PIN8(DB, kf, 0);
    PIN8(DB, kf, 8);
    }
    const u32 hd = lane & 3;
    u32x4 pf[2];
    float m2;
    SEC(4) {
    BAR();
    STAMP(6);
    if (V1 < 16) B_V(V1);
    f32x4 sc[4];
    {
      const u32x4 qf = lds_r128(L0 + QBI_ + hd * 256 + (((4 * tq + g) ^ (hd << 2)) << 4));
      const bool c8 = (lane & 8) != 0, c4 = (lane & 4) != 0;
#pragma unroll
      for (int t = 0; t < 4; ++t) {
        f32x4 d[4];
#pragma unroll
        for (int c = 0; c < 4; ++c) d[c] = mfma16(kf[4 * t + c], qf, f32x4{0.f, 0.f, 0.f, 0.f});
#pragma unroll
        for (int e = 0; e < 4; ++e) {
          const auto x = __builtin_amdgcn_permlane32_swap(asu(d[0][e]), asu(d[2][e]), false, false);
          const auto y = __builtin_amdgcn_permlane32_swap(asu(d[1][e]), asu(d[3][e]), false, false);
          const auto z = __builtin_amdgcn_permlane16_swap(c8 ? x[1] : x[0], c8 ? y[1] : y[0], false, false);
          float v = asf(c4 ? z[1] : z[0]);
          v += dppf<0x124>(v); v += dppf<0x128>(v);
          sc[t][e] = v;
        }
      }
    }
    float mx = -INFINITY;
#pragma unroll
    for (int t = 0; t < 4; ++t) mx = fmaxf(mx, fmaxf(fmaxf(sc[t][0], sc[t][1]), fmaxf(sc[t][2], sc[t][3])));
    mx = x32max(x16max(mx));
    if (lane < 4) lds_w32(L0 + MXB + lane * 32 + w * 4, asu(mx));
    BAR();
    if (V1 + 1 < 16) B_V(V1 + 1);
    mx = fmaxf(max4(lds_r128(L0 + MXB + hd * 32)), max4(lds_r128(L0 + MXB + hd * 32 + 16)));
    m2 = mx * C2;
    float ls = 0.f;
#pragma unroll
    for (int t = 0; t < 4; ++t) {
      const float p0 = ex2(sc[t][0] * C2 - m2), p1 = ex2(sc[t][1] * C2 - m2), p2 = ex2(sc[t][2] * C2 - m2), p3 = ex2(sc[t][3] * C2 - m2);
      ls += (p0 + p1) + (p2 + p3);
      pf[t >> 1][2 * (t & 1)] = pk(p0, p1); pf[t >> 1][2 * (t & 1) + 1] = pk(p2, p3);
    }
    ls = x32sum(x16sum(ls));
    if (lane < 4) lds_w32(L0 + LBB + lane * 32 + w * 4, asu(ls));
    STAMP(7);
    }
    f32x4 oa[2] = {f32x4{0.f, 0.f, 0.f, 0.f}, f32x4{0.f, 0.f, 0.f, 0.f}};
    u32x4 wb[4], xv, gv;
    constexpr int V2 = V1 + 2 < 16 ? V1 + 2 : 16, V3 = (8 + DB > V2) ? ((8 + DB < 16) ? 8 + DB : 16) : V2;
#pragma unroll
    for (int pi = 0; pi < 2; ++pi) SEC(5 + pi) {
      const int lo = (pi == 0) ? V2 : V3, hi = (pi == 0) ? V3 : 16;
#pragma unroll
      for (int i = lo; i < hi; ++i) { WAITV(DB - 1); B_V(i); }
      if (pi == (V3 < 16)) {
#pragma unroll
        for (int i = 0; i < 4; ++i) LD_NT(wb[i], g * 1024 + (w_vo & 255), wob + (size_t)i * 32768, 0);
        LD_DF(xv, lane * 16, p.x + w * 1024, 0); LD_DF(gv, lane * 16, p.gw, 0);
      }
      u32x4 pm[4];
#pragma unroll
      for (int gq = 0; gq < 4; ++gq)
#pragma unroll
        for (int e = 0; e < 4; ++e) pm[gq][e] = (tq == (u32)gq) ? pf[pi][e] : 0u;
      SB();
      WAITV(hi - 8 * (pi + 1) + 6 * (pi >= (V3 < 16)));
      if (pi == 1) STAMP(8);
      u32x4 va[8][1];
#pragma unroll
      for (int n = 0; n < 8; ++n) wfrag<1>(va[n], L0 + w * 16384 + pi * 8192, 2 * n, lane);
      SB();
#pragma unroll
      for (int n = 0; n < 8; ++n) oa[n & 1] = mfma16(pm[n >> 1], va[(n & 1) * 4 + (n >> 1)][0], oa[n & 1]);
    }
    SEC(7) {
#pragma unroll
    for (int u = 0; u < 2; ++u)
#pragma unroll
      for (int r = 0; r < 4; ++r) lds_w32(L0 + RED + w * 2048 + r * 512 + u * 256 + lane * 4, asu(oa[u][r]));
    BAR();
    if (w >= 4) {
      const int w3 = w & 3;
      float v0 = 0.f, v1 = 0.f;
      u32x2 t[8];
#pragma unroll
      for (int i = 0; i < 8; ++i) t[i] = lds_r64(L0 + RED + i * 2048 + w3 * 512 + lane * 8);
      const u32x4 l0 = lds_r128(L0 + LBB + w3 * 32), l1 = lds_r128(L0 + LBB + w3 * 32 + 16);
      SB();
#pragma unroll
      for (int i = 0; i < 8; ++i) { v0 += asf(t[i][0]); v1 += asf(t[i][1]); }
      const u32 slot = ((kvh * 4 + w3) * 8 + b) * 2 + hf;
      st_sc1(ws, WS_OB + slot * 1024 + lane * 16, u32x4{asu(v0), tag, asu(v1), tag});
      const float lt = sum4(l0) + sum4(l1);
      if (lane == (u32)w3) st_sc1(ws, WS_OBS + slot * 16, u32x4{asu(m2), tag, asu(lt), tag});
    }
    STAMP(9);
    PIN4(0, wb[0], wb[1], wb[2], wb[3]);
    PIN2(0, xv, gv);
    u32x4 ov[4];
    const u32 oo = WS_OB + (h2 * 8 + w) * 2048 + lane * 16, os = WS_OBS + (h2 * 8 + w) * 32;
    LD_SC1(ov[0], oo, ws, 0); LD_SC1(ov[1], oo, ws, 1024); LD_SC1(ov[2], os, ws, 0); LD_SC1(ov[3], os, ws, 16);
#pragma unroll
    for (int i = 0; i < 4; ++i) lds_w128(L0 + ((i * 8 + w) << 10) + lane * 16, wb[i]);
    const float gate = gate_of(xv, gv);
    PIN4(0, ov[0], ov[1], ov[2], ov[3]);
    for (int spin = 0; spin < SPIN_CAP; ++spin) {
      if (all_zero(tdiff(ov[0], tag) | tdiff(ov[1], tag) | tdiff(ov[2], tag) | tdiff(ov[3], tag))) break;
      RELOAD(ov[0], oo, 0); RELOAD(ov[1], oo, 1024); RELOAD(ov[2], os, 0); RELOAD(ov[3], os, 16);
      PIN4(0, ov[0], ov[1], ov[2], ov[3]);
    }
    {
      const float M = fmaxf(asf(ov[2][0]), asf(ov[3][0]));
      const float e0 = ex2(asf(ov[2][0]) - M), e1 = ex2(asf(ov[3][0]) - M);
      const float f = (1.f - gate) / (e0 * asf(ov[2][2]) + e1 * asf(ov[3][2]));
      lds_w32(L0 + QIM + w * 256 + (((lane >> 2) ^ (w << 1)) << 4) + (lane & 3) * 4,
              pk((e0 * asf(ov[0][0]) + e1 * asf(ov[1][0])) * f, (e0 * asf(ov[0][2]) + e1 * asf(ov[1][2])) * f));
    }
    BAR();
    {
      u32x4 w3[4], x3[4];
      afrag(x3, L0 + QIM, lane); wfrag<4>(w3, L0, 2 * w, lane);
      SB();
      const f32x4 oacc = mm4(w3, x3, f32x4{0.f, 0.f, 0.f, 0.f});
      st_sc1(ws, WS_OUTP + ((j * 32 + 4 * w + g) * 64 + 2 * h2 + 1) * 256 + (lane & 15) * 16, u32x4{asu(dup ? oacc[2] : oacc[0]), tag, asu(dup ? oacc[3] : oacc[1]), tag});
    }
    STAMP(13);
    }
    }
    STAMP_FLUSH();
  }
}

extern "C" void kernel_launch(void* const* d_in, const int* in_sizes, int n_in, void* d_out, int out_size, void* d_ws, size_t ws_size,
                              hipStream_t stream) {
  Params p;
  p.x = (const unsigned char*)d_in[0]; p.ckv = (const unsigned char*)d_in[1]; p.wq_a = (const unsigned char*)d_in[2];
  p.wuk = (const unsigned char*)d_in[3]; p.wuv = (const unsigned char*)d_in[4]; p.wo_a = (const unsigned char*)d_in[5];
  p.kc = (const unsigned char*)d_in[7]; p.vc = (const unsigned char*)d_in[8]; p.wq_b = (const unsigned char*)d_in[9];
  p.wo_b = (const unsigned char*)d_in[10]; p.gw = (const unsigned char*)d_in[11];
  p.ws = (unsigned char*)d_ws; p.out = (unsigned short*)d_out;
  (void)hipFuncSetAttribute((const void*)fused_decode, hipFuncAttributeMaxDynamicSharedMemorySize, LDS_BYTES);
  fused_decode<<<256, 512, LDS_BYTES, stream>>>(p);
}
```
